```python
import torch

def _softmax(x, axis=-1):
    return torch.softmax(x, dim=axis)
def _gelu(x):
    return torch.nn.functional.gelu(x, approximate="tanh")
def _silu(x):
    return torch.nn.functional.silu(x)
def _sigmoid(x):
    return torch.sigmoid(x)
def _layernorm(x, axis=-1, eps=1e-5):
    m = x.mean(dim=axis, keepdim=True)
    v = x.var(dim=axis, keepdim=True, unbiased=False)
    return (x - m) / torch.sqrt(v + eps)
def _scatter_add(tgt, idx, src, axis=0):
    return tgt.clone().index_add_(axis, idx, src)
def _segment_sum(src, seg, n):
    out = torch.zeros((n,) + src.shape[1:], dtype=src.dtype)
    return out.index_add_(0, seg, src)
def _pad(x, pw):
    flat = tuple(v for pair in reversed(pw) for v in pair)
    return torch.nn.functional.pad(x, flat)

kernel_name = 'parallel_sum__grouped_kv_mqa__dense_activation_squared_relu__prompt'

def setup_inputs():

    _w = (lambda a: (lambda t: t.to(torch.bfloat16) if t.is_floating_point() else t)(torch.as_tensor(a)))
    def _A_setup():
        B, S, D, H = 1, 2048, 640, 32
        Dh = D // H
        return {
            "x": _w((lambda *s: torch.randn(*s, dtype=torch.bfloat16) * 0.05)(B, S, D)),
            "wq": _w((lambda *s: torch.randn(*s, dtype=torch.bfloat16) * 0.2)(D, H * Dh)),
            "wk": _w((lambda *s: torch.randn(*s, dtype=torch.bfloat16) * 0.2)(D, Dh)),
            "wv": _w((lambda *s: torch.randn(*s, dtype=torch.bfloat16) * 0.1)(D, Dh)),
            "wo": _w((lambda *s: torch.randn(*s, dtype=torch.bfloat16) * 0.1)(H * Dh, D)),
        }
    _A_in = _A_setup()

    def _B_setup():
        B, S, D, F = 1, 2048, 640, 1536
        return {
            "x": _w((lambda *s: torch.randn(*s, dtype=torch.bfloat16) * 0.05)(B, S, D)),
            "w1": _w((lambda *s: torch.randn(*s, dtype=torch.bfloat16) * 0.1)(D, F)),
            "b1": _w((lambda *s: torch.randn(*s, dtype=torch.bfloat16) * 0.4)(F)),
            "w2": _w((lambda *s: torch.randn(*s, dtype=torch.bfloat16) * 0.1)(F, D)),
            "b2": _w((lambda *s: torch.randn(*s, dtype=torch.bfloat16) * 12.8)(D)),
        }
    _B_in = _B_setup()

    return {
        "A_x": _A_in["x"],
        "A_wq": _A_in["wq"],
        "A_wk": _A_in["wk"],
        "A_wv": _A_in["wv"],
        "A_wo": _A_in["wo"],
        "B_x": _B_in["x"],
        "B_w1": _B_in["w1"],
        "B_b1": _B_in["b1"],
        "B_w2": _B_in["w2"],
        "B_b2": _B_in["b2"],
    }

def reference(A_x, A_wq, A_wk, A_wv, A_wo, B_x, B_w1, B_b1, B_w2, B_b2):
    def _A_ref(x, wq, wk, wv, wo):
        B, S, D = x.shape
        H = 32
        Dh = D // H
        q = (lambda x, *d: x.permute(*d))((x @ wq).reshape(B, S, H, Dh), 0, 2, 1, 3)
        k = x @ wk
        v = x @ wv
        scores = torch.einsum("bhqd,bkd->bhqk", q, k) * (Dh ** -0.5)
        p = _softmax(scores, axis=-1)
        o = torch.einsum("bhqk,bkd->bhqd", p, v)
        return (lambda x, *d: x.permute(*d))(o, 0, 2, 1, 3).reshape(B, S, H * Dh) @ wo

    def _B_ref(x, w1, b1, w2, b2):
        h = torch.einsum("bsd,df->bsf", x, w1) + b1
        zero = torch.zeros_like(h)
        r = torch.where(h > zero, h, zero)
        h = r * r
        out = torch.einsum("bsf,fd->bsd", h, w2) + b2
        return out

    x = A_x
    xn = _layernorm(x, axis=-1)
    return x + _A_ref(xn, A_wq, A_wk, A_wv, A_wo) + _B_ref(xn, B_w1, B_b1, B_w2, B_b2)
```

```cpp
#include <hip/hip_runtime.h>
#include <hip/hip_bf16.h>
typedef unsigned short u16;
__device__ __forceinline__ float bf2f(u16 v){ return __uint_as_float(((unsigned)v)<<16); }
__device__ __forceinline__ u16 f2bf(float f){ unsigned u=__float_as_uint(f); u += 0x7fff + ((u>>16)&1); return (u16)(u>>16); }
constexpr int S=2048, D=640, H=32, DH=20, F=1536;
__global__ void k_ln(const u16* x, float* xn){
  int t=blockIdx.x; __shared__ float red[256];
  float s=0; for(int i=threadIdx.x;i<D;i+=256) s+=bf2f(x[t*D+i]);
  red[threadIdx.x]=s; __syncthreads();
  for(int o=128;o>0;o>>=1){ if(threadIdx.x<o) red[threadIdx.x]+=red[threadIdx.x+o]; __syncthreads(); }
  float m=red[0]/D; __syncthreads();
  float v=0; for(int i=threadIdx.x;i<D;i+=256){ float d=bf2f(x[t*D+i])-m; v+=d*d; }
  red[threadIdx.x]=v; __syncthreads();
  for(int o=128;o>0;o>>=1){ if(threadIdx.x<o) red[threadIdx.x]+=red[threadIdx.x+o]; __syncthreads(); }
  float r=1.0f/sqrtf(red[0]/D+1e-5f);
  for(int i=threadIdx.x;i<D;i+=256) xn[t*D+i]=bf2f(f2bf((bf2f(x[t*D+i])-m)*r));
}
__global__ void k_gemm(const float* A, const u16* W, float* C, int M,int N,int K, const u16* bias, int act){
  int n=blockIdx.x*64+threadIdx.x%64; int m=blockIdx.y*4+threadIdx.x/64;
  if(n>=N) return; float acc=0;
  for(int k=0;k<K;++k) acc+=A[(size_t)m*K+k]*bf2f(W[(size_t)k*N+n]);
  if(bias) acc+=bf2f(bias[n]);
  if(act){ acc=acc>0?acc:0; acc=acc*acc; }
  C[(size_t)m*N+n]=bf2f(f2bf(acc));
}
__global__ void k_attn(const float* q,const float* k,const float* v,float* o){
  int t=blockIdx.x, h=blockIdx.y; __shared__ float sc[S]; __shared__ float red[256];
  float qq[DH]; for(int d=0;d<DH;++d) qq[d]=q[t*D+h*DH+d];
  float mx=-1e30f;
  for(int j=threadIdx.x;j<S;j+=256){ float s=0; for(int d=0;d<DH;++d) s+=qq[d]*k[j*DH+d]; s*=0.22360679774997896f; sc[j]=s; mx=fmaxf(mx,s);}
  red[threadIdx.x]=mx; __syncthreads();
  for(int o2=128;o2>0;o2>>=1){ if(threadIdx.x<o2) red[threadIdx.x]=fmaxf(red[threadIdx.x],red[threadIdx.x+o2]); __syncthreads(); }
  mx=red[0]; __syncthreads();
  float sum=0; for(int j=threadIdx.x;j<S;j+=256){ float e=__expf(sc[j]-mx); sc[j]=e; sum+=e; }
  red[threadIdx.x]=sum; __syncthreads();
  for(int o2=128;o2>0;o2>>=1){ if(threadIdx.x<o2) red[threadIdx.x]+=red[threadIdx.x+o2]; __syncthreads(); }
  sum=red[0]; __syncthreads();
  if(threadIdx.x<DH){ float a=0; for(int j=0;j<S;++j) a+=sc[j]*v[j*DH+threadIdx.x]; o[t*D+h*DH+threadIdx.x]=bf2f(f2bf(a/sum)); }
}
__global__ void k_fin(const u16* x,const float* a,const float* b,u16* out){ int i=blockIdx.x*256+threadIdx.x; out[i]=f2bf(bf2f(x[i])+a[i]+b[i]); }
extern "C" void kernel_launch(void* const* d_in, const int* in_sizes, int n_in, void* d_out, int out_size, void* d_ws, size_t ws_size, hipStream_t stream){
  const u16* x=(const u16*)d_in[0]; const u16* wq=(const u16*)d_in[1]; const u16* wk=(const u16*)d_in[2]; const u16* wv=(const u16*)d_in[3]; const u16* wo=(const u16*)d_in[4];
  const u16* w1=(const u16*)d_in[6]; const u16* b1=(const u16*)d_in[7]; const u16* w2=(const u16*)d_in[8]; const u16* b2=(const u16*)d_in[9];
  float* ws=(float*)d_ws; float* xn=ws; float* q=xn+S*D; float* k=q+S*D; float* v=k+S*DH; float* o=v+S*DH; float* a=o+S*D; float* h=a+S*D; float* b=h+S*F;
  k_ln<<<S,256,0,stream>>>(x,xn);
  k_gemm<<<dim3(10,S/4),256,0,stream>>>(xn,wq,q,S,D,D,nullptr,0);
  k_gemm<<<dim3(1,S/4),256,0,stream>>>(xn,wk,k,S,DH,D,nullptr,0);
  k_gemm<<<dim3(1,S/4),256,0,stream>>>(xn,wv,v,S,DH,D,nullptr,0);
  k_attn<<<dim3(S,H),256,0,stream>>>(q,k,v,o);
  k_gemm<<<dim3(10,S/4),256,0,stream>>>(o,wo,a,S,D,D,nullptr,0);
  k_gemm<<<dim3(24,S/4),256,0,stream>>>(xn,w1,h,S,F,D,b1,1);
  k_gemm<<<dim3(10,S/4),256,0,stream>>>(h,w2,b,S,D,F,b2,0);
  k_fin<<<S*D/256,256,0,stream>>>(x,a,b,(u16*)d_out);
}
```

```cpp
#include <hip/hip_runtime.h>
#include <stdint.h>

typedef unsigned short u16;
typedef unsigned long long u64;
typedef float f32x16 __attribute__((ext_vector_type(16)));
typedef float f32x4 __attribute__((ext_vector_type(4)));
typedef float f32x8 __attribute__((ext_vector_type(8)));
typedef float f32x2 __attribute__((ext_vector_type(2)));
typedef short bf16x8 __attribute__((ext_vector_type(8)));
typedef short s16x4 __attribute__((ext_vector_type(4)));
typedef unsigned u32x4 __attribute__((ext_vector_type(4)));
typedef unsigned u32x2 __attribute__((ext_vector_type(2)));
typedef unsigned u32x8 __attribute__((ext_vector_type(8)));
typedef unsigned u32x16 __attribute__((ext_vector_type(16)));
#define DI __device__ __forceinline__
#define LAS __attribute__((address_space(3)))
#define GAS __attribute__((address_space(1)))
typedef LAS char* lptr;

#define MAPV 1
#define STOP_AFTER 99

constexpr float CSC = 0.22360679774997896f * 1.4426950408889634f;
constexpr float REF_OFF = 96.0f;

constexpr unsigned WS_E = 0, WS_FKV = 4096, WS_KV = 0x10000, KV_TILE = 4096, WS_FC = 0x80000, WS_FO = 0xC0000;
constexpr unsigned WS_HF = 0x100000, WS_OF = 0x700000, WS_WP = 0xA00000, WS_END = 0xD00000;
constexpr unsigned WS_DBG = 0x1000000;
constexpr unsigned XS_STR = 1296, RING = 82944, SLOT = 24576, MISC = 156672, LDS_BYTES = 163840;
constexpr unsigned SPIN_CAP = 1u << 15;
#define CODE_BYTES 30336u

DI unsigned cvtpk(float lo, float hi) { unsigned r; asm("v_cvt_pk_bf16_f32 %0, %1, %2" : "=v"(r) : "v"(lo), "v"(hi)); return r; }
DI float bf_lo(unsigned u) { return __uint_as_float(u << 16); }
DI float bf_hi(unsigned u) { return __uint_as_float(u & 0xffff0000u); }
DI float bf_round(float f) { return __uint_as_float(cvtpk(f, f) << 16); }
DI void bdma16(__amdgpu_buffer_rsrc_t rs, unsigned voff, unsigned soff, unsigned lds_dst) {
  unsigned keep;
  asm volatile("s_mov_b32 %0, m0\n\ts_mov_b32 m0, %4\n\ts_nop 0\n\tbuffer_load_dwordx4 %1, %2, %3 offen lds\n\ts_mov_b32 m0, %0" : "=&s"(keep) : "v"(voff), "s"(rs), "s"(soff), "s"(lds_dst) : "memory");
}
DI void bdma16_sc1(__amdgpu_buffer_rsrc_t rs, unsigned voff, unsigned soff, unsigned lds_dst) {
  unsigned keep;
  asm volatile("s_mov_b32 %0, m0\n\ts_mov_b32 m0, %4\n\ts_nop 0\n\tbuffer_load_dwordx4 %1, %2, %3 offen sc1 lds\n\ts_mov_b32 m0, %0" : "=&s"(keep) : "v"(voff), "s"(rs), "s"(soff), "s"(lds_dst) : "memory");
}
DI void bdma4(__amdgpu_buffer_rsrc_t rs, unsigned voff, unsigned lds_dst) {
  unsigned keep;
  asm volatile("s_mov_b32 %0, m0\n\ts_mov_b32 m0, %3\n\ts_nop 0\n\tbuffer_load_dword %1, %2, 0 offen lds\n\ts_mov_b32 m0, %0" : "=&s"(keep) : "v"(voff), "s"(rs), "s"(lds_dst) : "memory");
}
DI void glds16_pre(const char*& g, u64 stride, unsigned lds_dst) {
  unsigned keep;
  asm volatile("v_lshl_add_u64 %1, %1, 0, %2\n\ts_mov_b32 %0, m0\n\ts_mov_b32 m0, %3\n\ts_nop 0\n\tglobal_load_lds_dwordx4 %1, off\n\ts_mov_b32 m0, %0" : "=&s"(keep), "+v"(g) : "v"(stride), "s"(lds_dst) : "memory");
}
#define MKRS(p, n) __builtin_amdgcn_make_buffer_rsrc((void*)(p), 0, (int)(n), 0x00020000)
DI s16x4 trd(lptr p) { typedef short v4i16 __attribute__((ext_vector_type(4))); return __builtin_bit_cast(s16x4, __builtin_amdgcn_ds_read_tr16_b64_v4i16((LAS v4i16*)p)); }
DI bf16x8 cat8(s16x4 a, s16x4 b) { return __builtin_shufflevector(a, b, 0, 1, 2, 3, 4, 5, 6, 7); }
#define MF32(a, b, c) __builtin_amdgcn_mfma_f32_32x32x16_bf16((a), (b), (c), 0, 0, 0)
#define MF16(a, b, c) __builtin_amdgcn_mfma_f32_16x16x32_bf16((a), (b), (c), 0, 0, 0)
#define WAITV_BAR(n) asm volatile("s_waitcnt vmcnt(" #n ") lgkmcnt(0)\n\ts_barrier" ::: "memory")
#define BAR() asm volatile("s_waitcnt lgkmcnt(0)\n\ts_barrier" ::: "memory")
#define SB() __builtin_amdgcn_sched_barrier(0)
#define DRAIN() asm volatile("s_waitcnt vmcnt(0)" ::: "memory")
typedef __amdgpu_buffer_rsrc_t rsrc_t;
#define ST16(v, rs, off) __builtin_amdgcn_raw_buffer_store_b128((v), (rs), (int)(off), 0, 16)
#define ST8(v, rs, off) __builtin_amdgcn_raw_buffer_store_b64((v), (rs), (int)(off), 0, 16)
#define LD16(rs, off) __builtin_amdgcn_raw_buffer_load_b128((rs), (int)(off), 0, 16)
#define LD8(rs, off) __builtin_amdgcn_raw_buffer_load_b64((rs), (int)(off), 0, 16)

DI void wait_flags(rsrc_t rs, unsigned off, bool active, u64 tag) {
  const unsigned tl = (unsigned)tag, th = (unsigned)(tag >> 32);
#pragma unroll 1
  for (unsigned s = 0; s < SPIN_CAP; ++s) {
    u32x2 f = LD8(rs, off);
    bool ok = !active || (f.x == tl && f.y == th);
    if (__all(ok)) break;
    __builtin_amdgcn_s_sleep(2);
  }
}

template <int CTRL> DI float dppf(float v) { return __uint_as_float(__builtin_amdgcn_update_dpp(0u, __float_as_uint(v), CTRL, 0xf, 0xf, true)); }
DI float sum8(float v) { v += dppf<0xB1>(v); v += dppf<0x4E>(v); v += dppf<0x141>(v); return v; }
DI float max16(const f32x16& s) {
  float a = fmaxf(fmaxf(s[0], s[1]), s[2]), b = fmaxf(fmaxf(s[3], s[4]), s[5]);
  a = fmaxf(fmaxf(a, s[6]), s[7]); b = fmaxf(fmaxf(b, s[8]), s[9]);
  a = fmaxf(fmaxf(a, s[10]), s[11]); b = fmaxf(fmaxf(b, s[12]), s[13]);
  a = fmaxf(fmaxf(a, s[14]), s[15]);
  return fmaxf(a, b);
}

#define ATTN_ASM \
"s_mov_b32 s70, m0\n" \
"v_mov_b32 v152, 0\n" \
"v_mov_b32 v153, 0\n" \
"v_mov_b32 v154, 0\n" \
"v_mov_b32 v155, 0\n" \
"s_cmp_eq_u32 s73, 0\n" \
"s_cbranch_scc1 8f\n" \
"v_add_u32 v159, s73, v156\n" \
"ds_read_b128 v[64:67], v159\n" \
"ds_read_b64 v[68:69], v159 offset:1024\n" \
"s_mov_b32 s75, 0x07060302\n" \
"s_waitcnt lgkmcnt(0)\n" \
"v_mfma_f32_32x32x16_bf16 v[0:15], v[64:67], v[104:107], 0\n" \
"v_mfma_f32_32x32x16_bf16 v[16:31], v[64:67], v[108:111], 0\n" \
"v_mfma_f32_32x32x16_bf16 v[32:47], v[64:67], v[112:115], 0\n" \
"v_mfma_f32_32x32x16_bf16 v[48:63], v[64:67], v[116:119], 0\n" \
"v_mfma_f32_32x32x8_bf16 v[0:15], v[68:69], v[120:121], v[0:15]\n" \
"v_mfma_f32_32x32x8_bf16 v[16:31], v[68:69], v[122:123], v[16:31]\n" \
"v_mfma_f32_32x32x8_bf16 v[32:47], v[68:69], v[124:125], v[32:47]\n" \
"v_mfma_f32_32x32x8_bf16 v[48:63], v[68:69], v[126:127], v[48:63]\n" \
"s_nop 8\n" \
"v_max3_f32 v70, v0, v1, v2\n" \
"v_max3_f32 v71, v3, v4, v5\n" \
"v_max3_f32 v70, v70, v6, v7\n" \
"v_max3_f32 v71, v71, v8, v9\n" \
"v_max3_f32 v70, v70, v10, v11\n" \
"v_max3_f32 v71, v71, v12, v13\n" \
"v_max3_f32 v70, v70, v14, v15\n" \
"v_max_f32 v70, v70, v71\n" \
"v_max3_f32 v72, v16, v17, v18\n" \
"v_max3_f32 v73, v19, v20, v21\n" \
"v_max3_f32 v72, v72, v22, v23\n" \
"v_max3_f32 v73, v73, v24, v25\n" \
"v_max3_f32 v72, v72, v26, v27\n" \
"v_max3_f32 v73, v73, v28, v29\n" \
"v_max3_f32 v72, v72, v30, v31\n" \
"v_max_f32 v72, v72, v73\n" \
"v_max3_f32 v74, v32, v33, v34\n" \
"v_max3_f32 v75, v35, v36, v37\n" \
"v_max3_f32 v74, v74, v38, v39\n" \
"v_max3_f32 v75, v75, v40, v41\n" \
"v_max3_f32 v74, v74, v42, v43\n" \
"v_max3_f32 v75, v75, v44, v45\n" \
"v_max3_f32 v74, v74, v46, v47\n" \
"v_max_f32 v74, v74, v75\n" \
"v_max3_f32 v76, v48, v49, v50\n" \
"v_max3_f32 v77, v51, v52, v53\n" \
"v_max3_f32 v76, v76, v54, v55\n" \
"v_max3_f32 v77, v77, v56, v57\n" \
"v_max3_f32 v76, v76, v58, v59\n" \
"v_max3_f32 v77, v77, v60, v61\n" \
"v_max3_f32 v76, v76, v62, v63\n" \
"v_max_f32 v76, v76, v77\n" \
"v_mov_b32 v71, v70\n" \
"v_mov_b32 v73, v72\n" \
"v_mov_b32 v75, v74\n" \
"v_mov_b32 v77, v76\n" \
"s_nop 1\n" \
"v_permlane32_swap_b32 v70, v71\n" \
"v_permlane32_swap_b32 v72, v73\n" \
"v_permlane32_swap_b32 v74, v75\n" \
"v_permlane32_swap_b32 v76, v77\n" \
"v_max_f32 v70, v70, v71\n" \
"v_max_f32 v72, v72, v73\n" \
"v_max_f32 v74, v74, v75\n" \
"v_max_f32 v76, v76, v77\n" \
"v_sub_f32 v70, s74, v70\n" \
"v_sub_f32 v72, s74, v72\n" \
"v_sub_f32 v74, s74, v74\n" \
"v_sub_f32 v76, s74, v76\n" \
"v_and_b32 v71, 0xffff0000, v70\n" \
"v_and_b32 v73, 0xffff0000, v72\n" \
"v_and_b32 v75, 0xffff0000, v74\n" \
"v_and_b32 v77, 0xffff0000, v76\n" \
"v_sub_f32 v71, v70, v71\n" \
"v_sub_f32 v73, v72, v73\n" \
"v_sub_f32 v75, v74, v75\n" \
"v_sub_f32 v77, v76, v77\n" \
"v_and_b32 v80, 0xffff0000, v71\n" \
"v_and_b32 v81, 0xffff0000, v73\n" \
"v_and_b32 v82, 0xffff0000, v75\n" \
"v_and_b32 v83, 0xffff0000, v77\n" \
"v_sub_f32 v80, v71, v80\n" \
"v_sub_f32 v81, v73, v81\n" \
"v_sub_f32 v82, v75, v82\n" \
"v_sub_f32 v83, v77, v83\n" \
"s_mov_b32 exec_lo, 0\n" \
"v_perm_b32 v120, v71, v70, s75\n" \
"v_lshrrev_b32 v121, 16, v80\n" \
"v_perm_b32 v122, v73, v72, s75\n" \
"v_lshrrev_b32 v123, 16, v81\n" \
"v_perm_b32 v124, v75, v74, s75\n" \
"v_lshrrev_b32 v125, 16, v82\n" \
"v_perm_b32 v126, v77, v76, s75\n" \
"v_lshrrev_b32 v127, 16, v83\n" \
"s_mov_b32 exec_lo, -1\n" \
"8:\n" \
"v_mfma_f32_32x32x16_bf16 v[0:15], v[152:155], v[104:107], 0\n" \
"v_mfma_f32_32x32x16_bf16 v[16:31], v[152:155], v[108:111], 0\n" \
"v_mfma_f32_32x32x16_bf16 v[32:47], v[152:155], v[112:115], 0\n" \
"v_mfma_f32_32x32x16_bf16 v[48:63], v[152:155], v[116:119], 0\n" \
"v_mov_b32 v100, 0\n" \
"v_mov_b32 v101, 0\n" \
"v_mov_b32 v102, 0\n" \
"v_mov_b32 v103, 0\n" \
"v_mov_b32 v92, 0\n" \
"v_mov_b32 v93, 0\n" \
"v_mov_b32 v94, 0\n" \
"v_mov_b32 v95, 0\n" \
"v_mfma_f32_32x32x16_bf16 v[64:79], v[128:131], v[104:107], 0\n" \
"v_mfma_f32_32x32x8_bf16 v[64:79], v[132:133], v[120:121], v[64:79]\n" \
"s_nop 7\n" \
"s_nop 3\n" \
"s_mov_b32 s42, 4\n" \
"s_mov_b32 s71, 0\n" \
".p2align 6\n" \
"1:\n" \
"v_exp_f32 v64, v64\n" \
"v_exp_f32 v65, v65\n" \
"v_cvt_pk_bf16_f32 v102, v92, v93\n" \
"v_cvt_pk_bf16_f32 v103, v94, v95\n" \
"v_mfma_f32_32x32x16_bf16 v[80:95], v[128:131], v[108:111], 0\n" \
"v_exp_f32 v66, v66\n" \
"v_exp_f32 v67, v67\n" \
"v_exp_f32 v68, v68\n" \
"v_exp_f32 v69, v69\n" \
"v_cvt_pk_bf16_f32 v96, v64, v65\n" \
"v_cvt_pk_bf16_f32 v97, v66, v67\n" \
"v_mfma_f32_32x32x8_bf16 v[80:95], v[132:133], v[122:123], v[80:95]\n" \
"v_exp_f32 v70, v70\n" \
"v_exp_f32 v71, v71\n" \
"v_exp_f32 v72, v72\n" \
"v_exp_f32 v73, v73\n" \
"v_cvt_pk_bf16_f32 v98, v68, v69\n" \
"v_cvt_pk_bf16_f32 v99, v70, v71\n" \
"v_mfma_f32_32x32x16_bf16 v[48:63], v[152:155], v[100:103], v[48:63]\n" \
"buffer_load_dwordx4 v[148:151], v156, s[56:59], s40 offen offset:1024 sc1\n" \
"buffer_load_dwordx4 v[152:155], v156, s[56:59], s40 offen offset:2048 sc1\n" \
"s_add_u32 s40, s40, 4096\n" \
"s_cmp_lg_u32 s42, 1\n" \
"s_cbranch_scc1 7f\n" \
"buffer_load_dwordx2 v[158:159], v156, s[56:59], s44 offen sc1\n" \
"7:\n" \
"v_exp_f32 v74, v74\n" \
"v_exp_f32 v75, v75\n" \
"v_exp_f32 v76, v76\n" \
"v_exp_f32 v77, v77\n" \
"v_cvt_pk_bf16_f32 v100, v72, v73\n" \
"v_cvt_pk_bf16_f32 v101, v74, v75\n" \
"s_waitcnt vmcnt(4)\n" \
"v_mfma_f32_32x32x16_bf16 v[0:15], v[140:143], v[96:99], v[0:15]\n" \
"v_exp_f32 v78, v78\n" \
"v_exp_f32 v79, v79\n" \
"v_exp_f32 v80, v80\n" \
"v_exp_f32 v81, v81\n" \
"v_cvt_pk_bf16_f32 v102, v76, v77\n" \
"v_cvt_pk_bf16_f32 v103, v78, v79\n" \
"v_mfma_f32_32x32x16_bf16 v[64:79], v[128:131], v[112:115], 0\n" \
"v_exp_f32 v82, v82\n" \
"v_exp_f32 v83, v83\n" \
"v_exp_f32 v84, v84\n" \
"v_exp_f32 v85, v85\n" \
"v_cvt_pk_bf16_f32 v96, v80, v81\n" \
"v_cvt_pk_bf16_f32 v97, v82, v83\n" \
"v_mfma_f32_32x32x8_bf16 v[64:79], v[132:133], v[124:125], v[64:79]\n" \
"v_exp_f32 v86, v86\n" \
"v_exp_f32 v87, v87\n" \
"v_exp_f32 v88, v88\n" \
"v_exp_f32 v89, v89\n" \
"v_cvt_pk_bf16_f32 v98, v84, v85\n" \
"v_cvt_pk_bf16_f32 v99, v86, v87\n" \
"v_mfma_f32_32x32x16_bf16 v[0:15], v[144:147], v[100:103], v[0:15]\n" \
"v_exp_f32 v90, v90\n" \
"v_exp_f32 v91, v91\n" \
"v_exp_f32 v92, v92\n" \
"v_exp_f32 v93, v93\n" \
"v_cvt_pk_bf16_f32 v100, v88, v89\n" \
"v_cvt_pk_bf16_f32 v101, v90, v91\n" \
"v_mfma_f32_32x32x16_bf16 v[16:31], v[140:143], v[96:99], v[16:31]\n" \
"v_exp_f32 v94, v94\n" \
"v_exp_f32 v95, v95\n" \
"v_exp_f32 v64, v64\n" \
"v_exp_f32 v65, v65\n" \
"v_cvt_pk_bf16_f32 v102, v92, v93\n" \
"v_cvt_pk_bf16_f32 v103, v94, v95\n" \
"v_mfma_f32_32x32x16_bf16 v[80:95], v[128:131], v[116:119], 0\n" \
"v_exp_f32 v66, v66\n" \
"v_exp_f32 v67, v67\n" \
"v_exp_f32 v68, v68\n" \
"v_exp_f32 v69, v69\n" \
"v_cvt_pk_bf16_f32 v96, v64, v65\n" \
"v_cvt_pk_bf16_f32 v97, v66, v67\n" \
"v_mfma_f32_32x32x8_bf16 v[80:95], v[132:133], v[126:127], v[80:95]\n" \
"v_exp_f32 v70, v70\n" \
"v_exp_f32 v71, v71\n" \
"v_exp_f32 v72, v72\n" \
"v_exp_f32 v73, v73\n" \
"v_cvt_pk_bf16_f32 v98, v68, v69\n" \
"v_cvt_pk_bf16_f32 v99, v70, v71\n" \
"v_mfma_f32_32x32x16_bf16 v[16:31], v[144:147], v[100:103], v[16:31]\n" \
"v_exp_f32 v74, v74\n" \
"v_exp_f32 v75, v75\n" \
"v_exp_f32 v76, v76\n" \
"v_exp_f32 v77, v77\n" \
"v_cvt_pk_bf16_f32 v100, v72, v73\n" \
"v_cvt_pk_bf16_f32 v101, v74, v75\n" \
"v_mfma_f32_32x32x16_bf16 v[32:47], v[140:143], v[96:99], v[32:47]\n" \
"v_exp_f32 v78, v78\n" \
"v_exp_f32 v79, v79\n" \
"v_exp_f32 v80, v80\n" \
"v_exp_f32 v81, v81\n" \
"v_cvt_pk_bf16_f32 v102, v76, v77\n" \
"v_cvt_pk_bf16_f32 v103, v78, v79\n" \
"s_waitcnt vmcnt(2)\n" \
"v_mfma_f32_32x32x16_bf16 v[64:79], v[134:137], v[104:107], 0\n" \
"v_exp_f32 v82, v82\n" \
"v_exp_f32 v83, v83\n" \
"v_exp_f32 v84, v84\n" \
"v_exp_f32 v85, v85\n" \
"v_cvt_pk_bf16_f32 v96, v80, v81\n" \
"v_cvt_pk_bf16_f32 v97, v82, v83\n" \
"v_mfma_f32_32x32x8_bf16 v[64:79], v[138:139], v[120:121], v[64:79]\n" \
"buffer_load_dwordx4 v[128:131], v156, s[56:59], s40 offen sc1\n" \
"buffer_load_dwordx2 v[132:133], v156, s[56:59], s40 offen offset:3072 sc1\n" \
"v_exp_f32 v86, v86\n" \
"v_exp_f32 v87, v87\n" \
"v_exp_f32 v88, v88\n" \
"v_exp_f32 v89, v89\n" \
"v_cvt_pk_bf16_f32 v98, v84, v85\n" \
"v_cvt_pk_bf16_f32 v99, v86, v87\n" \
"v_mfma_f32_32x32x16_bf16 v[32:47], v[144:147], v[100:103], v[32:47]\n" \
"v_exp_f32 v90, v90\n" \
"v_exp_f32 v91, v91\n" \
"v_exp_f32 v92, v92\n" \
"v_exp_f32 v93, v93\n" \
"v_cvt_pk_bf16_f32 v100, v88, v89\n" \
"v_cvt_pk_bf16_f32 v101, v90, v91\n" \
"v_mfma_f32_32x32x16_bf16 v[48:63], v[140:143], v[96:99], v[48:63]\n" \
"v_exp_f32 v94, v94\n" \
"v_exp_f32 v95, v95\n" \
"v_exp_f32 v64, v64\n" \
"v_exp_f32 v65, v65\n" \
"v_cvt_pk_bf16_f32 v102, v92, v93\n" \
"v_cvt_pk_bf16_f32 v103, v94, v95\n" \
"v_mfma_f32_32x32x16_bf16 v[80:95], v[134:137], v[108:111], 0\n" \
"v_exp_f32 v66, v66\n" \
"v_exp_f32 v67, v67\n" \
"v_exp_f32 v68, v68\n" \
"v_exp_f32 v69, v69\n" \
"v_cvt_pk_bf16_f32 v96, v64, v65\n" \
"v_cvt_pk_bf16_f32 v97, v66, v67\n" \
"v_mfma_f32_32x32x8_bf16 v[80:95], v[138:139], v[122:123], v[80:95]\n" \
"v_exp_f32 v70, v70\n" \
"v_exp_f32 v71, v71\n" \
"v_exp_f32 v72, v72\n" \
"v_exp_f32 v73, v73\n" \
"v_cvt_pk_bf16_f32 v98, v68, v69\n" \
"v_cvt_pk_bf16_f32 v99, v70, v71\n" \
"v_mfma_f32_32x32x16_bf16 v[48:63], v[144:147], v[100:103], v[48:63]\n" \
"buffer_load_dwordx4 v[140:143], v156, s[56:59], s40 offen offset:1024 sc1\n" \
"buffer_load_dwordx4 v[144:147], v156, s[56:59], s40 offen offset:2048 sc1\n" \
"s_add_u32 s40, s40, 4096\n" \
"v_exp_f32 v74, v74\n" \
"v_exp_f32 v75, v75\n" \
"v_exp_f32 v76, v76\n" \
"v_exp_f32 v77, v77\n" \
"v_cvt_pk_bf16_f32 v100, v72, v73\n" \
"v_cvt_pk_bf16_f32 v101, v74, v75\n" \
"s_waitcnt vmcnt(4)\n" \
"v_mfma_f32_32x32x16_bf16 v[0:15], v[148:151], v[96:99], v[0:15]\n" \
"v_exp_f32 v78, v78\n" \
"v_exp_f32 v79, v79\n" \
"v_exp_f32 v80, v80\n" \
"v_exp_f32 v81, v81\n" \
"v_cvt_pk_bf16_f32 v102, v76, v77\n" \
"v_cvt_pk_bf16_f32 v103, v78, v79\n" \
"v_mfma_f32_32x32x16_bf16 v[64:79], v[134:137], v[112:115], 0\n" \
"v_exp_f32 v82, v82\n" \
"v_exp_f32 v83, v83\n" \
"v_exp_f32 v84, v84\n" \
"v_exp_f32 v85, v85\n" \
"v_cvt_pk_bf16_f32 v96, v80, v81\n" \
"v_cvt_pk_bf16_f32 v97, v82, v83\n" \
"v_mfma_f32_32x32x8_bf16 v[64:79], v[138:139], v[124:125], v[64:79]\n" \
"v_exp_f32 v86, v86\n" \
"v_exp_f32 v87, v87\n" \
"v_exp_f32 v88, v88\n" \
"v_exp_f32 v89, v89\n" \
"v_cvt_pk_bf16_f32 v98, v84, v85\n" \
"v_cvt_pk_bf16_f32 v99, v86, v87\n" \
"v_mfma_f32_32x32x16_bf16 v[0:15], v[152:155], v[100:103], v[0:15]\n" \
"v_exp_f32 v90, v90\n" \
"v_exp_f32 v91, v91\n" \
"v_exp_f32 v92, v92\n" \
"v_exp_f32 v93, v93\n" \
"v_cvt_pk_bf16_f32 v100, v88, v89\n" \
"v_cvt_pk_bf16_f32 v101, v90, v91\n" \
"v_mfma_f32_32x32x16_bf16 v[16:31], v[148:151], v[96:99], v[16:31]\n" \
"v_exp_f32 v94, v94\n" \
"v_exp_f32 v95, v95\n" \
"v_exp_f32 v64, v64\n" \
"v_exp_f32 v65, v65\n" \
"v_cvt_pk_bf16_f32 v102, v92, v93\n" \
"v_cvt_pk_bf16_f32 v103, v94, v95\n" \
"v_mfma_f32_32x32x16_bf16 v[80:95], v[134:137], v[116:119], 0\n" \
"v_exp_f32 v66, v66\n" \
"v_exp_f32 v67, v67\n" \
"v_exp_f32 v68, v68\n" \
"v_exp_f32 v69, v69\n" \
"v_cvt_pk_bf16_f32 v96, v64, v65\n" \
"v_cvt_pk_bf16_f32 v97, v66, v67\n" \
"v_mfma_f32_32x32x8_bf16 v[80:95], v[138:139], v[126:127], v[80:95]\n" \
"v_exp_f32 v70, v70\n" \
"v_exp_f32 v71, v71\n" \
"v_exp_f32 v72, v72\n" \
"v_exp_f32 v73, v73\n" \
"v_cvt_pk_bf16_f32 v98, v68, v69\n" \
"v_cvt_pk_bf16_f32 v99, v70, v71\n" \
"v_mfma_f32_32x32x16_bf16 v[16:31], v[152:155], v[100:103], v[16:31]\n" \
"v_exp_f32 v74, v74\n" \
"v_exp_f32 v75, v75\n" \
"v_exp_f32 v76, v76\n" \
"v_exp_f32 v77, v77\n" \
"v_cvt_pk_bf16_f32 v100, v72, v73\n" \
"v_cvt_pk_bf16_f32 v101, v74, v75\n" \
"v_mfma_f32_32x32x16_bf16 v[32:47], v[148:151], v[96:99], v[32:47]\n" \
"v_exp_f32 v78, v78\n" \
"v_exp_f32 v79, v79\n" \
"v_exp_f32 v80, v80\n" \
"v_exp_f32 v81, v81\n" \
"v_cvt_pk_bf16_f32 v102, v76, v77\n" \
"v_cvt_pk_bf16_f32 v103, v78, v79\n" \
"s_waitcnt vmcnt(2)\n" \
"v_mfma_f32_32x32x16_bf16 v[64:79], v[128:131], v[104:107], 0\n" \
"v_exp_f32 v82, v82\n" \
"v_exp_f32 v83, v83\n" \
"v_exp_f32 v84, v84\n" \
"v_exp_f32 v85, v85\n" \
"v_cvt_pk_bf16_f32 v96, v80, v81\n" \
"v_cvt_pk_bf16_f32 v97, v82, v83\n" \
"v_mfma_f32_32x32x8_bf16 v[64:79], v[132:133], v[120:121], v[64:79]\n" \
"buffer_load_dwordx4 v[134:137], v156, s[56:59], s40 offen sc1\n" \
"buffer_load_dwordx2 v[138:139], v156, s[56:59], s40 offen offset:3072 sc1\n" \
"v_exp_f32 v86, v86\n" \
"v_exp_f32 v87, v87\n" \
"v_exp_f32 v88, v88\n" \
"v_exp_f32 v89, v89\n" \
"v_cvt_pk_bf16_f32 v98, v84, v85\n" \
"v_cvt_pk_bf16_f32 v99, v86, v87\n" \
"v_mfma_f32_32x32x16_bf16 v[32:47], v[152:155], v[100:103], v[32:47]\n" \
"v_exp_f32 v90, v90\n" \
"v_exp_f32 v91, v91\n" \
"v_exp_f32 v92, v92\n" \
"v_exp_f32 v93, v93\n" \
"v_cvt_pk_bf16_f32 v100, v88, v89\n" \
"v_cvt_pk_bf16_f32 v101, v90, v91\n" \
"v_mfma_f32_32x32x16_bf16 v[48:63], v[148:151], v[96:99], v[48:63]\n" \
"v_exp_f32 v94, v94\n" \
"v_exp_f32 v95, v95\n" \
"s_add_u32 s71, s71, 1\n" \
"v_mov_b32 v161, s71\n" \
"s_mov_b64 exec, 1\n" \
"ds_write_b32 v160, v161\n" \
"ds_read_b32 v161, v162\n" \
"s_mov_b64 exec, -1\n" \
"s_waitcnt lgkmcnt(0)\n" \
"v_readfirstlane_b32 s72, v161\n" \
"s_nop 0\n" \
"s_cmp_gt_u32 s71, s72\n" \
"s_cbranch_scc1 910f\n" \
"s_setprio 1\n" \
"s_branch 911f\n" \
"910:\n" \
"s_setprio 0\n" \
"911:\n" \
"s_cmp_lg_u32 s42, 4\n" \
"s_cbranch_scc1 4f\n" \
"s_barrier\n" \
"s_cmp_lg_u32 s66, 0\n" \
"s_cbranch_scc1 4f\n" \
"v_lshlrev_b32 v168, 6, v156\n" \
"v_lshlrev_b32 v169, 9, v156\n" \
"v_cmp_gt_u32 vcc, 0x80, v156\n" \
"v_add_u32 v168, s64, v168\n" \
"v_add_u32 v169, s65, v169\n" \
"v_mov_b32 v170, s46\n" \
"v_mov_b32 v171, s47\n" \
"v_cndmask_b32 v168, v169, v168, vcc\n" \
"s_mov_b32 exec_hi, 0xff\n" \
"buffer_store_dwordx2 v[170:171], v168, s[56:59], 0 offen sc1\n" \
"s_mov_b32 exec_hi, -1\n" \
"4:\n" \
"s_sub_u32 s42, s42, 1\n" \
"s_cmp_lg_u32 s42, 0\n" \
"s_cbranch_scc1 1b\n" \
"s_waitcnt vmcnt(6)\n" \
"s_mov_b32 s67, 0x8000\n" \
"2:\n" \
"v_cmp_eq_u32 vcc, s46, v158\n" \
"v_cmp_eq_u32 s[68:69], s47, v159\n" \
"s_nop 1\n" \
"s_and_b64 vcc, vcc, s[68:69]\n" \
"s_or_b32 vcc_hi, vcc_hi, 0xffffff00\n" \
"s_cmp_eq_u64 vcc, -1\n" \
"s_cbranch_scc1 3f\n" \
"s_sub_u32 s67, s67, 1\n" \
"s_cmp_eq_u32 s67, 0\n" \
"s_cbranch_scc1 3f\n" \
"s_sleep 2\n" \
"buffer_load_dwordx2 v[158:159], v156, s[56:59], s44 offen sc1\n" \
"s_waitcnt vmcnt(0)\n" \
"s_branch 2b\n" \
"3:\n" \
"s_mov_b32 m0, s63\n" \
"s_nop 0\n" \
"buffer_load_dword v157, s[56:59], 0 offen sc1 lds\n" \
"v_exp_f32 v64, v64\n" \
"v_exp_f32 v65, v65\n" \
"v_cvt_pk_bf16_f32 v102, v92, v93\n" \
"v_cvt_pk_bf16_f32 v103, v94, v95\n" \
"v_mfma_f32_32x32x16_bf16 v[80:95], v[128:131], v[108:111], 0\n" \
"v_exp_f32 v66, v66\n" \
"v_exp_f32 v67, v67\n" \
"v_exp_f32 v68, v68\n" \
"v_exp_f32 v69, v69\n" \
"v_cvt_pk_bf16_f32 v96, v64, v65\n" \
"v_cvt_pk_bf16_f32 v97, v66, v67\n" \
"v_mfma_f32_32x32x8_bf16 v[80:95], v[132:133], v[122:123], v[80:95]\n" \
"v_exp_f32 v70, v70\n" \
"v_exp_f32 v71, v71\n" \
"v_exp_f32 v72, v72\n" \
"v_exp_f32 v73, v73\n" \
"v_cvt_pk_bf16_f32 v98, v68, v69\n" \
"v_cvt_pk_bf16_f32 v99, v70, v71\n" \
"v_mfma_f32_32x32x16_bf16 v[48:63], v[152:155], v[100:103], v[48:63]\n" \
"buffer_load_dwordx4 v[148:151], v156, s[56:59], s40 offen offset:1024 sc1\n" \
"buffer_load_dwordx4 v[152:155], v156, s[56:59], s40 offen offset:2048 sc1\n" \
"s_add_u32 s40, s40, 4096\n" \
"buffer_load_dwordx4 v[168:171], v156, s[56:59], s48 offen offset:0 sc1\n" \
"buffer_load_dwordx4 v[172:175], v156, s[56:59], s49 offen offset:0 sc1\n" \
"s_mov_b32 m0, s62\n" \
"s_nop 0\n" \
"buffer_load_dwordx4 v156, s[56:59], s52 offen offset:0 sc1 lds\n" \
"v_exp_f32 v74, v74\n" \
"v_exp_f32 v75, v75\n" \
"v_exp_f32 v76, v76\n" \
"v_exp_f32 v77, v77\n" \
"v_cvt_pk_bf16_f32 v100, v72, v73\n" \
"v_cvt_pk_bf16_f32 v101, v74, v75\n" \
"s_waitcnt vmcnt(8)\n" \
"v_mfma_f32_32x32x16_bf16 v[0:15], v[140:143], v[96:99], v[0:15]\n" \
"v_exp_f32 v78, v78\n" \
"v_exp_f32 v79, v79\n" \
"v_exp_f32 v80, v80\n" \
"v_exp_f32 v81, v81\n" \
"v_cvt_pk_bf16_f32 v102, v76, v77\n" \
"v_cvt_pk_bf16_f32 v103, v78, v79\n" \
"v_mfma_f32_32x32x16_bf16 v[64:79], v[128:131], v[112:115], 0\n" \
"v_exp_f32 v82, v82\n" \
"v_exp_f32 v83, v83\n" \
"v_exp_f32 v84, v84\n" \
"v_exp_f32 v85, v85\n" \
"v_cvt_pk_bf16_f32 v96, v80, v81\n" \
"v_cvt_pk_bf16_f32 v97, v82, v83\n" \
"v_mfma_f32_32x32x8_bf16 v[64:79], v[132:133], v[124:125], v[64:79]\n" \
"v_exp_f32 v86, v86\n" \
"v_exp_f32 v87, v87\n" \
"v_exp_f32 v88, v88\n" \
"v_exp_f32 v89, v89\n" \
"v_cvt_pk_bf16_f32 v98, v84, v85\n" \
"v_cvt_pk_bf16_f32 v99, v86, v87\n" \
"v_mfma_f32_32x32x16_bf16 v[0:15], v[144:147], v[100:103], v[0:15]\n" \
"v_exp_f32 v90, v90\n" \
"v_exp_f32 v91, v91\n" \
"v_exp_f32 v92, v92\n" \
"v_exp_f32 v93, v93\n" \
"v_cvt_pk_bf16_f32 v100, v88, v89\n" \
"v_cvt_pk_bf16_f32 v101, v90, v91\n" \
"v_mfma_f32_32x32x16_bf16 v[16:31], v[140:143], v[96:99], v[16:31]\n" \
"v_exp_f32 v94, v94\n" \
"v_exp_f32 v95, v95\n" \
"v_exp_f32 v64, v64\n" \
"v_exp_f32 v65, v65\n" \
"v_cvt_pk_bf16_f32 v102, v92, v93\n" \
"v_cvt_pk_bf16_f32 v103, v94, v95\n" \
"v_mfma_f32_32x32x16_bf16 v[80:95], v[128:131], v[116:119], 0\n" \
"v_exp_f32 v66, v66\n" \
"v_exp_f32 v67, v67\n" \
"v_exp_f32 v68, v68\n" \
"v_exp_f32 v69, v69\n" \
"v_cvt_pk_bf16_f32 v96, v64, v65\n" \
"v_cvt_pk_bf16_f32 v97, v66, v67\n" \
"v_mfma_f32_32x32x8_bf16 v[80:95], v[132:133], v[126:127], v[80:95]\n" \
"v_exp_f32 v70, v70\n" \
"v_exp_f32 v71, v71\n" \
"v_exp_f32 v72, v72\n" \
"v_exp_f32 v73, v73\n" \
"v_cvt_pk_bf16_f32 v98, v68, v69\n" \
"v_cvt_pk_bf16_f32 v99, v70, v71\n" \
"v_mfma_f32_32x32x16_bf16 v[16:31], v[144:147], v[100:103], v[16:31]\n" \
"v_exp_f32 v74, v74\n" \
"v_exp_f32 v75, v75\n" \
"v_exp_f32 v76, v76\n" \
"v_exp_f32 v77, v77\n" \
"v_cvt_pk_bf16_f32 v100, v72, v73\n" \
"v_cvt_pk_bf16_f32 v101, v74, v75\n" \
"v_mfma_f32_32x32x16_bf16 v[32:47], v[140:143], v[96:99], v[32:47]\n" \
"v_exp_f32 v78, v78\n" \
"v_exp_f32 v79, v79\n" \
"v_exp_f32 v80, v80\n" \
"v_exp_f32 v81, v81\n" \
"v_cvt_pk_bf16_f32 v102, v76, v77\n" \
"v_cvt_pk_bf16_f32 v103, v78, v79\n" \
"s_waitcnt vmcnt(6)\n" \
"v_mfma_f32_32x32x16_bf16 v[64:79], v[134:137], v[104:107], 0\n" \
"v_exp_f32 v82, v82\n" \
"v_exp_f32 v83, v83\n" \
"v_exp_f32 v84, v84\n" \
"v_exp_f32 v85, v85\n" \
"v_cvt_pk_bf16_f32 v96, v80, v81\n" \
"v_cvt_pk_bf16_f32 v97, v82, v83\n" \
"v_mfma_f32_32x32x8_bf16 v[64:79], v[138:139], v[120:121], v[64:79]\n" \
"buffer_load_dwordx4 v[128:131], v156, s[56:59], s40 offen sc1\n" \
"buffer_load_dwordx2 v[132:133], v156, s[56:59], s40 offen offset:3072 sc1\n" \
"buffer_load_dwordx4 v[176:179], v156, s[56:59], s48 offen offset:1024 sc1\n" \
"buffer_load_dwordx4 v156, s[56:59], s52 offen offset:1024 sc1 lds\n" \
"v_exp_f32 v86, v86\n" \
"v_exp_f32 v87, v87\n" \
"v_exp_f32 v88, v88\n" \
"v_exp_f32 v89, v89\n" \
"v_cvt_pk_bf16_f32 v98, v84, v85\n" \
"v_cvt_pk_bf16_f32 v99, v86, v87\n" \
"v_mfma_f32_32x32x16_bf16 v[32:47], v[144:147], v[100:103], v[32:47]\n" \
"v_exp_f32 v90, v90\n" \
"v_exp_f32 v91, v91\n" \
"v_exp_f32 v92, v92\n" \
"v_exp_f32 v93, v93\n" \
"v_cvt_pk_bf16_f32 v100, v88, v89\n" \
"v_cvt_pk_bf16_f32 v101, v90, v91\n" \
"v_mfma_f32_32x32x16_bf16 v[48:63], v[140:143], v[96:99], v[48:63]\n" \
"v_exp_f32 v94, v94\n" \
"v_exp_f32 v95, v95\n" \
"v_exp_f32 v64, v64\n" \
"v_exp_f32 v65, v65\n" \
"v_cvt_pk_bf16_f32 v102, v92, v93\n" \
"v_cvt_pk_bf16_f32 v103, v94, v95\n" \
"v_mfma_f32_32x32x16_bf16 v[80:95], v[134:137], v[108:111], 0\n" \
"v_exp_f32 v66, v66\n" \
"v_exp_f32 v67, v67\n" \
"v_exp_f32 v68, v68\n" \
"v_exp_f32 v69, v69\n" \
"v_cvt_pk_bf16_f32 v96, v64, v65\n" \
"v_cvt_pk_bf16_f32 v97, v66, v67\n" \
"v_mfma_f32_32x32x8_bf16 v[80:95], v[138:139], v[122:123], v[80:95]\n" \
"v_exp_f32 v70, v70\n" \
"v_exp_f32 v71, v71\n" \
"v_exp_f32 v72, v72\n" \
"v_exp_f32 v73, v73\n" \
"v_cvt_pk_bf16_f32 v98, v68, v69\n" \
"v_cvt_pk_bf16_f32 v99, v70, v71\n" \
"v_mfma_f32_32x32x16_bf16 v[48:63], v[144:147], v[100:103], v[48:63]\n" \
"buffer_load_dwordx4 v[140:143], v156, s[56:59], s40 offen offset:1024 sc1\n" \
"buffer_load_dwordx4 v[144:147], v156, s[56:59], s40 offen offset:2048 sc1\n" \
"s_add_u32 s40, s40, 4096\n" \
"buffer_load_dwordx4 v[180:183], v156, s[56:59], s49 offen offset:1024 sc1\n" \
"buffer_load_dwordx4 v[184:187], v156, s[56:59], s48 offen offset:2048 sc1\n" \
"buffer_load_dwordx4 v156, s[56:59], s52 offen offset:2048 sc1 lds\n" \
"v_exp_f32 v74, v74\n" \
"v_exp_f32 v75, v75\n" \
"v_exp_f32 v76, v76\n" \
"v_exp_f32 v77, v77\n" \
"v_cvt_pk_bf16_f32 v100, v72, v73\n" \
"v_cvt_pk_bf16_f32 v101, v74, v75\n" \
"s_waitcnt vmcnt(12)\n" \
"v_mfma_f32_32x32x16_bf16 v[0:15], v[148:151], v[96:99], v[0:15]\n" \
"v_exp_f32 v78, v78\n" \
"v_exp_f32 v79, v79\n" \
"v_exp_f32 v80, v80\n" \
"v_exp_f32 v81, v81\n" \
"v_cvt_pk_bf16_f32 v102, v76, v77\n" \
"v_cvt_pk_bf16_f32 v103, v78, v79\n" \
"v_mfma_f32_32x32x16_bf16 v[64:79], v[134:137], v[112:115], 0\n" \
"v_exp_f32 v82, v82\n" \
"v_exp_f32 v83, v83\n" \
"v_exp_f32 v84, v84\n" \
"v_exp_f32 v85, v85\n" \
"v_cvt_pk_bf16_f32 v96, v80, v81\n" \
"v_cvt_pk_bf16_f32 v97, v82, v83\n" \
"v_mfma_f32_32x32x8_bf16 v[64:79], v[138:139], v[124:125], v[64:79]\n" \
"v_exp_f32 v86, v86\n" \
"v_exp_f32 v87, v87\n" \
"v_exp_f32 v88, v88\n" \
"v_exp_f32 v89, v89\n" \
"v_cvt_pk_bf16_f32 v98, v84, v85\n" \
"v_cvt_pk_bf16_f32 v99, v86, v87\n" \
"v_mfma_f32_32x32x16_bf16 v[0:15], v[152:155], v[100:103], v[0:15]\n" \
"v_exp_f32 v90, v90\n" \
"v_exp_f32 v91, v91\n" \
"v_exp_f32 v92, v92\n" \
"v_exp_f32 v93, v93\n" \
"v_cvt_pk_bf16_f32 v100, v88, v89\n" \
"v_cvt_pk_bf16_f32 v101, v90, v91\n" \
"v_mfma_f32_32x32x16_bf16 v[16:31], v[148:151], v[96:99], v[16:31]\n" \
"v_exp_f32 v94, v94\n" \
"v_exp_f32 v95, v95\n" \
"v_exp_f32 v64, v64\n" \
"v_exp_f32 v65, v65\n" \
"v_cvt_pk_bf16_f32 v102, v92, v93\n" \
"v_cvt_pk_bf16_f32 v103, v94, v95\n" \
"v_mfma_f32_32x32x16_bf16 v[80:95], v[134:137], v[116:119], 0\n" \
"v_exp_f32 v66, v66\n" \
"v_exp_f32 v67, v67\n" \
"v_exp_f32 v68, v68\n" \
"v_exp_f32 v69, v69\n" \
"v_cvt_pk_bf16_f32 v96, v64, v65\n" \
"v_cvt_pk_bf16_f32 v97, v66, v67\n" \
"v_mfma_f32_32x32x8_bf16 v[80:95], v[138:139], v[126:127], v[80:95]\n" \
"v_exp_f32 v70, v70\n" \
"v_exp_f32 v71, v71\n" \
"v_exp_f32 v72, v72\n" \
"v_exp_f32 v73, v73\n" \
"v_cvt_pk_bf16_f32 v98, v68, v69\n" \
"v_cvt_pk_bf16_f32 v99, v70, v71\n" \
"v_mfma_f32_32x32x16_bf16 v[16:31], v[152:155], v[100:103], v[16:31]\n" \
"v_exp_f32 v74, v74\n" \
"v_exp_f32 v75, v75\n" \
"v_exp_f32 v76, v76\n" \
"v_exp_f32 v77, v77\n" \
"v_cvt_pk_bf16_f32 v100, v72, v73\n" \
"v_cvt_pk_bf16_f32 v101, v74, v75\n" \
"v_mfma_f32_32x32x16_bf16 v[32:47], v[148:151], v[96:99], v[32:47]\n" \
"v_exp_f32 v78, v78\n" \
"v_exp_f32 v79, v79\n" \
"v_exp_f32 v80, v80\n" \
"v_exp_f32 v81, v81\n" \
"v_cvt_pk_bf16_f32 v102, v76, v77\n" \
"v_cvt_pk_bf16_f32 v103, v78, v79\n" \
"s_waitcnt vmcnt(7)\n" \
"v_mfma_f32_32x32x16_bf16 v[64:79], v[128:131], v[104:107], 0\n" \
"v_exp_f32 v82, v82\n" \
"v_exp_f32 v83, v83\n" \
"v_exp_f32 v84, v84\n" \
"v_exp_f32 v85, v85\n" \
"v_cvt_pk_bf16_f32 v96, v80, v81\n" \
"v_cvt_pk_bf16_f32 v97, v82, v83\n" \
"v_mfma_f32_32x32x8_bf16 v[64:79], v[132:133], v[120:121], v[64:79]\n" \
"buffer_load_dwordx4 v[134:137], v156, s[56:59], s40 offen sc1\n" \
"buffer_load_dwordx2 v[138:139], v156, s[56:59], s40 offen offset:3072 sc1\n" \
"buffer_load_dwordx4 v[188:191], v156, s[56:59], s49 offen offset:2048 sc1\n" \
"buffer_load_dwordx4 v156, s[56:59], s52 offen offset:3072 sc1 lds\n" \
"v_exp_f32 v86, v86\n" \
"v_exp_f32 v87, v87\n" \
"v_exp_f32 v88, v88\n" \
"v_exp_f32 v89, v89\n" \
"v_cvt_pk_bf16_f32 v98, v84, v85\n" \
"v_cvt_pk_bf16_f32 v99, v86, v87\n" \
"v_mfma_f32_32x32x16_bf16 v[32:47], v[152:155], v[100:103], v[32:47]\n" \
"v_exp_f32 v90, v90\n" \
"v_exp_f32 v91, v91\n" \
"v_exp_f32 v92, v92\n" \
"v_exp_f32 v93, v93\n" \
"v_cvt_pk_bf16_f32 v100, v88, v89\n" \
"v_cvt_pk_bf16_f32 v101, v90, v91\n" \
"v_mfma_f32_32x32x16_bf16 v[48:63], v[148:151], v[96:99], v[48:63]\n" \
"v_exp_f32 v94, v94\n" \
"v_exp_f32 v95, v95\n" \
"s_add_u32 s71, s71, 1\n" \
"v_mov_b32 v161, s71\n" \
"s_mov_b64 exec, 1\n" \
"ds_write_b32 v160, v161\n" \
"ds_read_b32 v161, v162\n" \
"s_mov_b64 exec, -1\n" \
"s_waitcnt lgkmcnt(0)\n" \
"v_readfirstlane_b32 s72, v161\n" \
"s_nop 0\n" \
"s_cmp_gt_u32 s71, s72\n" \
"s_cbranch_scc1 1090f\n" \
"s_setprio 1\n" \
"s_branch 1091f\n" \
"1090:\n" \
"s_setprio 0\n" \
"1091:\n" \
"v_exp_f32 v64, v64\n" \
"v_exp_f32 v65, v65\n" \
"v_cvt_pk_bf16_f32 v102, v92, v93\n" \
"v_cvt_pk_bf16_f32 v103, v94, v95\n" \
"v_mfma_f32_32x32x16_bf16 v[80:95], v[128:131], v[108:111], 0\n" \
"v_exp_f32 v66, v66\n" \
"v_exp_f32 v67, v67\n" \
"v_exp_f32 v68, v68\n" \
"v_exp_f32 v69, v69\n" \
"v_cvt_pk_bf16_f32 v96, v64, v65\n" \
"v_cvt_pk_bf16_f32 v97, v66, v67\n" \
"v_mfma_f32_32x32x8_bf16 v[80:95], v[132:133], v[122:123], v[80:95]\n" \
"v_exp_f32 v70, v70\n" \
"v_exp_f32 v71, v71\n" \
"v_exp_f32 v72, v72\n" \
"v_exp_f32 v73, v73\n" \
"v_cvt_pk_bf16_f32 v98, v68, v69\n" \
"v_cvt_pk_bf16_f32 v99, v70, v71\n" \
"v_mfma_f32_32x32x16_bf16 v[48:63], v[152:155], v[100:103], v[48:63]\n" \
"buffer_load_dwordx4 v[148:151], v156, s[56:59], s40 offen offset:1024 sc1\n" \
"buffer_load_dwordx4 v[152:155], v156, s[56:59], s40 offen offset:2048 sc1\n" \
"s_add_u32 s40, s40, 4096\n" \
"buffer_load_dwordx4 v[192:195], v156, s[56:59], s48 offen offset:3072 sc1\n" \
"buffer_load_dwordx4 v[196:199], v156, s[56:59], s49 offen offset:3072 sc1\n" \
"s_add_u32 s52, s52, 4096\n" \
"s_add_u32 m0, m0, 4096\n" \
"s_nop 0\n" \
"buffer_load_dwordx4 v156, s[56:59], s52 offen offset:0 sc1 lds\n" \
"v_exp_f32 v74, v74\n" \
"v_exp_f32 v75, v75\n" \
"v_exp_f32 v76, v76\n" \
"v_exp_f32 v77, v77\n" \
"v_cvt_pk_bf16_f32 v100, v72, v73\n" \
"v_cvt_pk_bf16_f32 v101, v74, v75\n" \
"s_waitcnt vmcnt(12)\n" \
"v_mfma_f32_32x32x16_bf16 v[0:15], v[140:143], v[96:99], v[0:15]\n" \
"v_exp_f32 v78, v78\n" \
"v_exp_f32 v79, v79\n" \
"v_exp_f32 v80, v80\n" \
"v_exp_f32 v81, v81\n" \
"v_cvt_pk_bf16_f32 v102, v76, v77\n" \
"v_cvt_pk_bf16_f32 v103, v78, v79\n" \
"v_mfma_f32_32x32x16_bf16 v[64:79], v[128:131], v[112:115], 0\n" \
"v_exp_f32 v82, v82\n" \
"v_exp_f32 v83, v83\n" \
"v_exp_f32 v84, v84\n" \
"v_exp_f32 v85, v85\n" \
"v_cvt_pk_bf16_f32 v96, v80, v81\n" \
"v_cvt_pk_bf16_f32 v97, v82, v83\n" \
"v_mfma_f32_32x32x8_bf16 v[64:79], v[132:133], v[124:125], v[64:79]\n" \
"v_exp_f32 v86, v86\n" \
"v_exp_f32 v87, v87\n" \
"v_exp_f32 v88, v88\n" \
"v_exp_f32 v89, v89\n" \
"v_cvt_pk_bf16_f32 v98, v84, v85\n" \
"v_cvt_pk_bf16_f32 v99, v86, v87\n" \
"v_mfma_f32_32x32x16_bf16 v[0:15], v[144:147], v[100:103], v[0:15]\n" \
"v_exp_f32 v90, v90\n" \
"v_exp_f32 v91, v91\n" \
"v_exp_f32 v92, v92\n" \
"v_exp_f32 v93, v93\n" \
"v_cvt_pk_bf16_f32 v100, v88, v89\n" \
"v_cvt_pk_bf16_f32 v101, v90, v91\n" \
"v_mfma_f32_32x32x16_bf16 v[16:31], v[140:143], v[96:99], v[16:31]\n" \
"v_exp_f32 v94, v94\n" \
"v_exp_f32 v95, v95\n" \
"v_exp_f32 v64, v64\n" \
"v_exp_f32 v65, v65\n" \
"v_cvt_pk_bf16_f32 v102, v92, v93\n" \
"v_cvt_pk_bf16_f32 v103, v94, v95\n" \
"v_mfma_f32_32x32x16_bf16 v[80:95], v[128:131], v[116:119], 0\n" \
"v_exp_f32 v66, v66\n" \
"v_exp_f32 v67, v67\n" \
"v_exp_f32 v68, v68\n" \
"v_exp_f32 v69, v69\n" \
"v_cvt_pk_bf16_f32 v96, v64, v65\n" \
"v_cvt_pk_bf16_f32 v97, v66, v67\n" \
"v_mfma_f32_32x32x8_bf16 v[80:95], v[132:133], v[126:127], v[80:95]\n" \
"v_exp_f32 v70, v70\n" \
"v_exp_f32 v71, v71\n" \
"v_exp_f32 v72, v72\n" \
"v_exp_f32 v73, v73\n" \
"v_cvt_pk_bf16_f32 v98, v68, v69\n" \
"v_cvt_pk_bf16_f32 v99, v70, v71\n" \
"v_mfma_f32_32x32x16_bf16 v[16:31], v[144:147], v[100:103], v[16:31]\n" \
"v_exp_f32 v74, v74\n" \
"v_exp_f32 v75, v75\n" \
"v_exp_f32 v76, v76\n" \
"v_exp_f32 v77, v77\n" \
"v_cvt_pk_bf16_f32 v100, v72, v73\n" \
"v_cvt_pk_bf16_f32 v101, v74, v75\n" \
"v_mfma_f32_32x32x16_bf16 v[32:47], v[140:143], v[96:99], v[32:47]\n" \
"v_exp_f32 v78, v78\n" \
"v_exp_f32 v79, v79\n" \
"v_exp_f32 v80, v80\n" \
"v_exp_f32 v81, v81\n" \
"v_cvt_pk_bf16_f32 v102, v76, v77\n" \
"v_cvt_pk_bf16_f32 v103, v78, v79\n" \
"s_waitcnt vmcnt(7)\n" \
"v_mfma_f32_32x32x16_bf16 v[64:79], v[134:137], v[104:107], 0\n" \
"v_exp_f32 v82, v82\n" \
"v_exp_f32 v83, v83\n" \
"v_exp_f32 v84, v84\n" \
"v_exp_f32 v85, v85\n" \
"v_cvt_pk_bf16_f32 v96, v80, v81\n" \
"v_cvt_pk_bf16_f32 v97, v82, v83\n" \
"v_mfma_f32_32x32x8_bf16 v[64:79], v[138:139], v[120:121], v[64:79]\n" \
"buffer_load_dwordx4 v[128:131], v156, s[56:59], s40 offen sc1\n" \
"buffer_load_dwordx2 v[132:133], v156, s[56:59], s40 offen offset:3072 sc1\n" \
"s_add_u32 s48, s48, 4096\n" \
"buffer_load_dwordx4 v[200:203], v156, s[56:59], s48 offen offset:0 sc1\n" \
"v_exp_f32 v86, v86\n" \
"v_exp_f32 v87, v87\n" \
"v_exp_f32 v88, v88\n" \
"v_exp_f32 v89, v89\n" \
"v_cvt_pk_bf16_f32 v98, v84, v85\n" \
"v_cvt_pk_bf16_f32 v99, v86, v87\n" \
"v_mfma_f32_32x32x16_bf16 v[32:47], v[144:147], v[100:103], v[32:47]\n" \
"v_exp_f32 v90, v90\n" \
"v_exp_f32 v91, v91\n" \
"v_exp_f32 v92, v92\n" \
"v_exp_f32 v93, v93\n" \
"v_cvt_pk_bf16_f32 v100, v88, v89\n" \
"v_cvt_pk_bf16_f32 v101, v90, v91\n" \
"v_mfma_f32_32x32x16_bf16 v[48:63], v[140:143], v[96:99], v[48:63]\n" \
"v_exp_f32 v94, v94\n" \
"v_exp_f32 v95, v95\n" \
"v_exp_f32 v64, v64\n" \
"v_exp_f32 v65, v65\n" \
"v_cvt_pk_bf16_f32 v102, v92, v93\n" \
"v_cvt_pk_bf16_f32 v103, v94, v95\n" \
"v_mfma_f32_32x32x16_bf16 v[80:95], v[134:137], v[108:111], 0\n" \
"v_exp_f32 v66, v66\n" \
"v_exp_f32 v67, v67\n" \
"v_exp_f32 v68, v68\n" \
"v_exp_f32 v69, v69\n" \
"v_cvt_pk_bf16_f32 v96, v64, v65\n" \
"v_cvt_pk_bf16_f32 v97, v66, v67\n" \
"v_mfma_f32_32x32x8_bf16 v[80:95], v[138:139], v[122:123], v[80:95]\n" \
"v_exp_f32 v70, v70\n" \
"v_exp_f32 v71, v71\n" \
"v_exp_f32 v72, v72\n" \
"v_exp_f32 v73, v73\n" \
"v_cvt_pk_bf16_f32 v98, v68, v69\n" \
"v_cvt_pk_bf16_f32 v99, v70, v71\n" \
"v_mfma_f32_32x32x16_bf16 v[48:63], v[144:147], v[100:103], v[48:63]\n" \
"buffer_load_dwordx4 v[140:143], v156, s[56:59], s40 offen offset:1024 sc1\n" \
"buffer_load_dwordx4 v[144:147], v156, s[56:59], s40 offen offset:2048 sc1\n" \
"s_add_u32 s40, s40, 4096\n" \
"s_add_u32 s49, s49, 4096\n" \
"buffer_load_dwordx4 v[204:207], v156, s[56:59], s49 offen offset:0 sc1\n" \
"buffer_load_dwordx4 v[208:211], v156, s[56:59], s48 offen offset:1024 sc1\n" \
"s_add_u32 s52, s52, 6144\n" \
"s_add_u32 m0, m0, 36864\n" \
"s_nop 0\n" \
"buffer_load_dwordx4 v156, s[56:59], s52 offen offset:0 sc1 lds\n" \
"v_exp_f32 v74, v74\n" \
"v_exp_f32 v75, v75\n" \
"v_exp_f32 v76, v76\n" \
"v_exp_f32 v77, v77\n" \
"v_cvt_pk_bf16_f32 v100, v72, v73\n" \
"v_cvt_pk_bf16_f32 v101, v74, v75\n" \
"s_waitcnt vmcnt(11)\n" \
"v_mfma_f32_32x32x16_bf16 v[0:15], v[148:151], v[96:99], v[0:15]\n" \
"v_exp_f32 v78, v78\n" \
"v_exp_f32 v79, v79\n" \
"v_exp_f32 v80, v80\n" \
"v_exp_f32 v81, v81\n" \
"v_cvt_pk_bf16_f32 v102, v76, v77\n" \
"v_cvt_pk_bf16_f32 v103, v78, v79\n" \
"v_mfma_f32_32x32x16_bf16 v[64:79], v[134:137], v[112:115], 0\n" \
"v_exp_f32 v82, v82\n" \
"v_exp_f32 v83, v83\n" \
"v_exp_f32 v84, v84\n" \
"v_exp_f32 v85, v85\n" \
"v_cvt_pk_bf16_f32 v96, v80, v81\n" \
"v_cvt_pk_bf16_f32 v97, v82, v83\n" \
"v_mfma_f32_32x32x8_bf16 v[64:79], v[138:139], v[124:125], v[64:79]\n" \
"v_exp_f32 v86, v86\n" \
"v_exp_f32 v87, v87\n" \
"v_exp_f32 v88, v88\n" \
"v_exp_f32 v89, v89\n" \
"v_cvt_pk_bf16_f32 v98, v84, v85\n" \
"v_cvt_pk_bf16_f32 v99, v86, v87\n" \
"v_mfma_f32_32x32x16_bf16 v[0:15], v[152:155], v[100:103], v[0:15]\n" \
"v_exp_f32 v90, v90\n" \
"v_exp_f32 v91, v91\n" \
"v_exp_f32 v92, v92\n" \
"v_exp_f32 v93, v93\n" \
"v_cvt_pk_bf16_f32 v100, v88, v89\n" \
"v_cvt_pk_bf16_f32 v101, v90, v91\n" \
"v_mfma_f32_32x32x16_bf16 v[16:31], v[148:151], v[96:99], v[16:31]\n" \
"v_exp_f32 v94, v94\n" \
"v_exp_f32 v95, v95\n" \
"v_exp_f32 v64, v64\n" \
"v_exp_f32 v65, v65\n" \
"v_cvt_pk_bf16_f32 v102, v92, v93\n" \
"v_cvt_pk_bf16_f32 v103, v94, v95\n" \
"v_mfma_f32_32x32x16_bf16 v[80:95], v[134:137], v[116:119], 0\n" \
"v_exp_f32 v66, v66\n" \
"v_exp_f32 v67, v67\n" \
"v_exp_f32 v68, v68\n" \
"v_exp_f32 v69, v69\n" \
"v_cvt_pk_bf16_f32 v96, v64, v65\n" \
"v_cvt_pk_bf16_f32 v97, v66, v67\n" \
"v_mfma_f32_32x32x8_bf16 v[80:95], v[138:139], v[126:127], v[80:95]\n" \
"v_exp_f32 v70, v70\n" \
"v_exp_f32 v71, v71\n" \
"v_exp_f32 v72, v72\n" \
"v_exp_f32 v73, v73\n" \
"v_cvt_pk_bf16_f32 v98, v68, v69\n" \
"v_cvt_pk_bf16_f32 v99, v70, v71\n" \
"v_mfma_f32_32x32x16_bf16 v[16:31], v[152:155], v[100:103], v[16:31]\n" \
"v_exp_f32 v74, v74\n" \
"v_exp_f32 v75, v75\n" \
"v_exp_f32 v76, v76\n" \
"v_exp_f32 v77, v77\n" \
"v_cvt_pk_bf16_f32 v100, v72, v73\n" \
"v_cvt_pk_bf16_f32 v101, v74, v75\n" \
"v_mfma_f32_32x32x16_bf16 v[32:47], v[148:151], v[96:99], v[32:47]\n" \
"v_exp_f32 v78, v78\n" \
"v_exp_f32 v79, v79\n" \
"v_exp_f32 v80, v80\n" \
"v_exp_f32 v81, v81\n" \
"v_cvt_pk_bf16_f32 v102, v76, v77\n" \
"v_cvt_pk_bf16_f32 v103, v78, v79\n" \
"s_waitcnt vmcnt(6)\n" \
"v_mfma_f32_32x32x16_bf16 v[64:79], v[128:131], v[104:107], 0\n" \
"v_exp_f32 v82, v82\n" \
"v_exp_f32 v83, v83\n" \
"v_exp_f32 v84, v84\n" \
"v_exp_f32 v85, v85\n" \
"v_cvt_pk_bf16_f32 v96, v80, v81\n" \
"v_cvt_pk_bf16_f32 v97, v82, v83\n" \
"v_mfma_f32_32x32x8_bf16 v[64:79], v[132:133], v[120:121], v[64:79]\n" \
"buffer_load_dwordx4 v[134:137], v156, s[56:59], s40 offen sc1\n" \
"buffer_load_dwordx2 v[138:139], v156, s[56:59], s40 offen offset:3072 sc1\n" \
"buffer_load_dwordx4 v[212:215], v156, s[56:59], s49 offen offset:1024 sc1\n" \
"v_exp_f32 v86, v86\n" \
"v_exp_f32 v87, v87\n" \
"v_exp_f32 v88, v88\n" \
"v_exp_f32 v89, v89\n" \
"v_cvt_pk_bf16_f32 v98, v84, v85\n" \
"v_cvt_pk_bf16_f32 v99, v86, v87\n" \
"v_mfma_f32_32x32x16_bf16 v[32:47], v[152:155], v[100:103], v[32:47]\n" \
"v_exp_f32 v90, v90\n" \
"v_exp_f32 v91, v91\n" \
"v_exp_f32 v92, v92\n" \
"v_exp_f32 v93, v93\n" \
"v_cvt_pk_bf16_f32 v100, v88, v89\n" \
"v_cvt_pk_bf16_f32 v101, v90, v91\n" \
"v_mfma_f32_32x32x16_bf16 v[48:63], v[148:151], v[96:99], v[48:63]\n" \
"v_exp_f32 v94, v94\n" \
"v_exp_f32 v95, v95\n" \
"s_add_u32 s71, s71, 1\n" \
"v_mov_b32 v161, s71\n" \
"s_mov_b64 exec, 1\n" \
"ds_write_b32 v160, v161\n" \
"ds_read_b32 v161, v162\n" \
"s_mov_b64 exec, -1\n" \
"s_waitcnt lgkmcnt(0)\n" \
"v_readfirstlane_b32 s72, v161\n" \
"s_nop 0\n" \
"s_cmp_gt_u32 s71, s72\n" \
"s_cbranch_scc1 1110f\n" \
"s_setprio 1\n" \
"s_branch 1111f\n" \
"1110:\n" \
"s_setprio 0\n" \
"1111:\n" \
"v_exp_f32 v64, v64\n" \
"v_exp_f32 v65, v65\n" \
"v_cvt_pk_bf16_f32 v102, v92, v93\n" \
"v_cvt_pk_bf16_f32 v103, v94, v95\n" \
"v_mfma_f32_32x32x16_bf16 v[80:95], v[128:131], v[108:111], 0\n" \
"v_exp_f32 v66, v66\n" \
"v_exp_f32 v67, v67\n" \
"v_exp_f32 v68, v68\n" \
"v_exp_f32 v69, v69\n" \
"v_cvt_pk_bf16_f32 v96, v64, v65\n" \
"v_cvt_pk_bf16_f32 v97, v66, v67\n" \
"v_mfma_f32_32x32x8_bf16 v[80:95], v[132:133], v[122:123], v[80:95]\n" \
"v_exp_f32 v70, v70\n" \
"v_exp_f32 v71, v71\n" \
"v_exp_f32 v72, v72\n" \
"v_exp_f32 v73, v73\n" \
"v_cvt_pk_bf16_f32 v98, v68, v69\n" \
"v_cvt_pk_bf16_f32 v99, v70, v71\n" \
"v_mfma_f32_32x32x16_bf16 v[48:63], v[152:155], v[100:103], v[48:63]\n" \
"buffer_load_dwordx4 v[148:151], v156, s[56:59], s40 offen offset:1024 sc1\n" \
"buffer_load_dwordx4 v[152:155], v156, s[56:59], s40 offen offset:2048 sc1\n" \
"s_add_u32 s40, s40, 4096\n" \
"buffer_load_dwordx4 v[216:219], v156, s[56:59], s48 offen offset:2048 sc1\n" \
"buffer_load_dwordx4 v[220:223], v156, s[56:59], s49 offen offset:2048 sc1\n" \
"buffer_load_dwordx4 v156, s[56:59], s52 offen offset:1024 sc1 lds\n" \
"v_exp_f32 v74, v74\n" \
"v_exp_f32 v75, v75\n" \
"v_exp_f32 v76, v76\n" \
"v_exp_f32 v77, v77\n" \
"v_cvt_pk_bf16_f32 v100, v72, v73\n" \
"v_cvt_pk_bf16_f32 v101, v74, v75\n" \
"s_waitcnt vmcnt(11)\n" \
"v_mfma_f32_32x32x16_bf16 v[0:15], v[140:143], v[96:99], v[0:15]\n" \
"v_exp_f32 v78, v78\n" \
"v_exp_f32 v79, v79\n" \
"v_exp_f32 v80, v80\n" \
"v_exp_f32 v81, v81\n" \
"v_cvt_pk_bf16_f32 v102, v76, v77\n" \
"v_cvt_pk_bf16_f32 v103, v78, v79\n" \
"v_mfma_f32_32x32x16_bf16 v[64:79], v[128:131], v[112:115], 0\n" \
"v_exp_f32 v82, v82\n" \
"v_exp_f32 v83, v83\n" \
"v_exp_f32 v84, v84\n" \
"v_exp_f32 v85, v85\n" \
"v_cvt_pk_bf16_f32 v96, v80, v81\n" \
"v_cvt_pk_bf16_f32 v97, v82, v83\n" \
"v_mfma_f32_32x32x8_bf16 v[64:79], v[132:133], v[124:125], v[64:79]\n" \
"v_exp_f32 v86, v86\n" \
"v_exp_f32 v87, v87\n" \
"v_exp_f32 v88, v88\n" \
"v_exp_f32 v89, v89\n" \
"v_cvt_pk_bf16_f32 v98, v84, v85\n" \
"v_cvt_pk_bf16_f32 v99, v86, v87\n" \
"v_mfma_f32_32x32x16_bf16 v[0:15], v[144:147], v[100:103], v[0:15]\n" \
"v_exp_f32 v90, v90\n" \
"v_exp_f32 v91, v91\n" \
"v_exp_f32 v92, v92\n" \
"v_exp_f32 v93, v93\n" \
"v_cvt_pk_bf16_f32 v100, v88, v89\n" \
"v_cvt_pk_bf16_f32 v101, v90, v91\n" \
"v_mfma_f32_32x32x16_bf16 v[16:31], v[140:143], v[96:99], v[16:31]\n" \
"v_exp_f32 v94, v94\n" \
"v_exp_f32 v95, v95\n" \
"v_exp_f32 v64, v64\n" \
"v_exp_f32 v65, v65\n" \
"v_cvt_pk_bf16_f32 v102, v92, v93\n" \
"v_cvt_pk_bf16_f32 v103, v94, v95\n" \
"v_mfma_f32_32x32x16_bf16 v[80:95], v[128:131], v[116:119], 0\n" \
"v_exp_f32 v66, v66\n" \
"v_exp_f32 v67, v67\n" \
"v_exp_f32 v68, v68\n" \
"v_exp_f32 v69, v69\n" \
"v_cvt_pk_bf16_f32 v96, v64, v65\n" \
"v_cvt_pk_bf16_f32 v97, v66, v67\n" \
"v_mfma_f32_32x32x8_bf16 v[80:95], v[132:133], v[126:127], v[80:95]\n" \
"v_exp_f32 v70, v70\n" \
"v_exp_f32 v71, v71\n" \
"v_exp_f32 v72, v72\n" \
"v_exp_f32 v73, v73\n" \
"v_cvt_pk_bf16_f32 v98, v68, v69\n" \
"v_cvt_pk_bf16_f32 v99, v70, v71\n" \
"v_mfma_f32_32x32x16_bf16 v[16:31], v[144:147], v[100:103], v[16:31]\n" \
"v_exp_f32 v74, v74\n" \
"v_exp_f32 v75, v75\n" \
"v_exp_f32 v76, v76\n" \
"v_exp_f32 v77, v77\n" \
"v_cvt_pk_bf16_f32 v100, v72, v73\n" \
"v_cvt_pk_bf16_f32 v101, v74, v75\n" \
"v_mfma_f32_32x32x16_bf16 v[32:47], v[140:143], v[96:99], v[32:47]\n" \
"v_exp_f32 v78, v78\n" \
"v_exp_f32 v79, v79\n" \
"v_exp_f32 v80, v80\n" \
"v_exp_f32 v81, v81\n" \
"v_cvt_pk_bf16_f32 v102, v76, v77\n" \
"v_cvt_pk_bf16_f32 v103, v78, v79\n" \
"s_waitcnt vmcnt(6)\n" \
"v_mfma_f32_32x32x16_bf16 v[64:79], v[134:137], v[104:107], 0\n" \
"v_exp_f32 v82, v82\n" \
"v_exp_f32 v83, v83\n" \
"v_exp_f32 v84, v84\n" \
"v_exp_f32 v85, v85\n" \
"v_cvt_pk_bf16_f32 v96, v80, v81\n" \
"v_cvt_pk_bf16_f32 v97, v82, v83\n" \
"v_mfma_f32_32x32x8_bf16 v[64:79], v[138:139], v[120:121], v[64:79]\n" \
"buffer_load_dwordx4 v[128:131], v156, s[56:59], s40 offen sc1\n" \
"buffer_load_dwordx2 v[132:133], v156, s[56:59], s40 offen offset:3072 sc1\n" \
"buffer_load_dwordx4 v[224:227], v156, s[56:59], s48 offen offset:3072 sc1\n" \
"v_exp_f32 v86, v86\n" \
"v_exp_f32 v87, v87\n" \
"v_exp_f32 v88, v88\n" \
"v_exp_f32 v89, v89\n" \
"v_cvt_pk_bf16_f32 v98, v84, v85\n" \
"v_cvt_pk_bf16_f32 v99, v86, v87\n" \
"v_mfma_f32_32x32x16_bf16 v[32:47], v[144:147], v[100:103], v[32:47]\n" \
"v_exp_f32 v90, v90\n" \
"v_exp_f32 v91, v91\n" \
"v_exp_f32 v92, v92\n" \
"v_exp_f32 v93, v93\n" \
"v_cvt_pk_bf16_f32 v100, v88, v89\n" \
"v_cvt_pk_bf16_f32 v101, v90, v91\n" \
"v_mfma_f32_32x32x16_bf16 v[48:63], v[140:143], v[96:99], v[48:63]\n" \
"v_exp_f32 v94, v94\n" \
"v_exp_f32 v95, v95\n" \
"v_exp_f32 v64, v64\n" \
"v_exp_f32 v65, v65\n" \
"v_cvt_pk_bf16_f32 v102, v92, v93\n" \
"v_cvt_pk_bf16_f32 v103, v94, v95\n" \
"v_mfma_f32_32x32x16_bf16 v[80:95], v[134:137], v[108:111], 0\n" \
"v_exp_f32 v66, v66\n" \
"v_exp_f32 v67, v67\n" \
"v_exp_f32 v68, v68\n" \
"v_exp_f32 v69, v69\n" \
"v_cvt_pk_bf16_f32 v96, v64, v65\n" \
"v_cvt_pk_bf16_f32 v97, v66, v67\n" \
"v_mfma_f32_32x32x8_bf16 v[80:95], v[138:139], v[122:123], v[80:95]\n" \
"v_exp_f32 v70, v70\n" \
"v_exp_f32 v71, v71\n" \
"v_exp_f32 v72, v72\n" \
"v_exp_f32 v73, v73\n" \
"v_cvt_pk_bf16_f32 v98, v68, v69\n" \
"v_cvt_pk_bf16_f32 v99, v70, v71\n" \
"v_mfma_f32_32x32x16_bf16 v[48:63], v[144:147], v[100:103], v[48:63]\n" \
"buffer_load_dwordx4 v[140:143], v156, s[56:59], s40 offen offset:1024 sc1\n" \
"buffer_load_dwordx4 v[144:147], v156, s[56:59], s40 offen offset:2048 sc1\n" \
"s_add_u32 s40, s40, 4096\n" \
"buffer_load_dwordx4 v[228:231], v156, s[56:59], s49 offen offset:3072 sc1\n" \
"s_add_u32 s48, s48, 4096\n" \
"buffer_load_dwordx4 v[232:235], v156, s[56:59], s48 offen offset:0 sc1\n" \
"buffer_load_dwordx4 v156, s[56:59], s52 offen offset:2048 sc1 lds\n" \
"v_exp_f32 v74, v74\n" \
"v_exp_f32 v75, v75\n" \
"v_exp_f32 v76, v76\n" \
"v_exp_f32 v77, v77\n" \
"v_cvt_pk_bf16_f32 v100, v72, v73\n" \
"v_cvt_pk_bf16_f32 v101, v74, v75\n" \
"s_waitcnt vmcnt(11)\n" \
"v_mfma_f32_32x32x16_bf16 v[0:15], v[148:151], v[96:99], v[0:15]\n" \
"v_exp_f32 v78, v78\n" \
"v_exp_f32 v79, v79\n" \
"v_exp_f32 v80, v80\n" \
"v_exp_f32 v81, v81\n" \
"v_cvt_pk_bf16_f32 v102, v76, v77\n" \
"v_cvt_pk_bf16_f32 v103, v78, v79\n" \
"v_mfma_f32_32x32x16_bf16 v[64:79], v[134:137], v[112:115], 0\n" \
"v_exp_f32 v82, v82\n" \
"v_exp_f32 v83, v83\n" \
"v_exp_f32 v84, v84\n" \
"v_exp_f32 v85, v85\n" \
"v_cvt_pk_bf16_f32 v96, v80, v81\n" \
"v_cvt_pk_bf16_f32 v97, v82, v83\n" \
"v_mfma_f32_32x32x8_bf16 v[64:79], v[138:139], v[124:125], v[64:79]\n" \
"v_exp_f32 v86, v86\n" \
"v_exp_f32 v87, v87\n" \
"v_exp_f32 v88, v88\n" \
"v_exp_f32 v89, v89\n" \
"v_cvt_pk_bf16_f32 v98, v84, v85\n" \
"v_cvt_pk_bf16_f32 v99, v86, v87\n" \
"v_mfma_f32_32x32x16_bf16 v[0:15], v[152:155], v[100:103], v[0:15]\n" \
"v_exp_f32 v90, v90\n" \
"v_exp_f32 v91, v91\n" \
"v_exp_f32 v92, v92\n" \
"v_exp_f32 v93, v93\n" \
"v_cvt_pk_bf16_f32 v100, v88, v89\n" \
"v_cvt_pk_bf16_f32 v101, v90, v91\n" \
"v_mfma_f32_32x32x16_bf16 v[16:31], v[148:151], v[96:99], v[16:31]\n" \
"v_exp_f32 v94, v94\n" \
"v_exp_f32 v95, v95\n" \
"v_exp_f32 v64, v64\n" \
"v_exp_f32 v65, v65\n" \
"v_cvt_pk_bf16_f32 v102, v92, v93\n" \
"v_cvt_pk_bf16_f32 v103, v94, v95\n" \
"v_mfma_f32_32x32x16_bf16 v[80:95], v[134:137], v[116:119], 0\n" \
"v_exp_f32 v66, v66\n" \
"v_exp_f32 v67, v67\n" \
"v_exp_f32 v68, v68\n" \
"v_exp_f32 v69, v69\n" \
"v_cvt_pk_bf16_f32 v96, v64, v65\n" \
"v_cvt_pk_bf16_f32 v97, v66, v67\n" \
"v_mfma_f32_32x32x8_bf16 v[80:95], v[138:139], v[126:127], v[80:95]\n" \
"v_exp_f32 v70, v70\n" \
"v_exp_f32 v71, v71\n" \
"v_exp_f32 v72, v72\n" \
"v_exp_f32 v73, v73\n" \
"v_cvt_pk_bf16_f32 v98, v68, v69\n" \
"v_cvt_pk_bf16_f32 v99, v70, v71\n" \
"v_mfma_f32_32x32x16_bf16 v[16:31], v[152:155], v[100:103], v[16:31]\n" \
"v_exp_f32 v74, v74\n" \
"v_exp_f32 v75, v75\n" \
"v_exp_f32 v76, v76\n" \
"v_exp_f32 v77, v77\n" \
"v_cvt_pk_bf16_f32 v100, v72, v73\n" \
"v_cvt_pk_bf16_f32 v101, v74, v75\n" \
"v_mfma_f32_32x32x16_bf16 v[32:47], v[148:151], v[96:99], v[32:47]\n" \
"v_exp_f32 v78, v78\n" \
"v_exp_f32 v79, v79\n" \
"v_exp_f32 v80, v80\n" \
"v_exp_f32 v81, v81\n" \
"v_cvt_pk_bf16_f32 v102, v76, v77\n" \
"v_cvt_pk_bf16_f32 v103, v78, v79\n" \
"s_waitcnt vmcnt(6)\n" \
"v_mfma_f32_32x32x16_bf16 v[64:79], v[128:131], v[104:107], 0\n" \
"v_exp_f32 v82, v82\n" \
"v_exp_f32 v83, v83\n" \
"v_exp_f32 v84, v84\n" \
"v_exp_f32 v85, v85\n" \
"v_cvt_pk_bf16_f32 v96, v80, v81\n" \
"v_cvt_pk_bf16_f32 v97, v82, v83\n" \
"v_mfma_f32_32x32x8_bf16 v[64:79], v[132:133], v[120:121], v[64:79]\n" \
"buffer_load_dwordx4 v[134:137], v156, s[56:59], s40 offen sc1\n" \
"buffer_load_dwordx2 v[138:139], v156, s[56:59], s40 offen offset:3072 sc1\n" \
"s_add_u32 s49, s49, 4096\n" \
"buffer_load_dwordx4 v[236:239], v156, s[56:59], s49 offen offset:0 sc1\n" \
"v_exp_f32 v86, v86\n" \
"v_exp_f32 v87, v87\n" \
"v_exp_f32 v88, v88\n" \
"v_exp_f32 v89, v89\n" \
"v_cvt_pk_bf16_f32 v98, v84, v85\n" \
"v_cvt_pk_bf16_f32 v99, v86, v87\n" \
"v_mfma_f32_32x32x16_bf16 v[32:47], v[152:155], v[100:103], v[32:47]\n" \
"v_exp_f32 v90, v90\n" \
"v_exp_f32 v91, v91\n" \
"v_exp_f32 v92, v92\n" \
"v_exp_f32 v93, v93\n" \
"v_cvt_pk_bf16_f32 v100, v88, v89\n" \
"v_cvt_pk_bf16_f32 v101, v90, v91\n" \
"v_mfma_f32_32x32x16_bf16 v[48:63], v[148:151], v[96:99], v[48:63]\n" \
"v_exp_f32 v94, v94\n" \
"v_exp_f32 v95, v95\n" \
"s_add_u32 s71, s71, 1\n" \
"v_mov_b32 v161, s71\n" \
"s_mov_b64 exec, 1\n" \
"ds_write_b32 v160, v161\n" \
"ds_read_b32 v161, v162\n" \
"s_mov_b64 exec, -1\n" \
"s_waitcnt lgkmcnt(0)\n" \
"v_readfirstlane_b32 s72, v161\n" \
"s_nop 0\n" \
"s_cmp_gt_u32 s71, s72\n" \
"s_cbranch_scc1 1130f\n" \
"s_setprio 1\n" \
"s_branch 1131f\n" \
"1130:\n" \
"s_setprio 0\n" \
"1131:\n" \
"v_exp_f32 v64, v64\n" \
"v_exp_f32 v65, v65\n" \
"v_cvt_pk_bf16_f32 v102, v92, v93\n" \
"v_cvt_pk_bf16_f32 v103, v94, v95\n" \
"v_mfma_f32_32x32x16_bf16 v[80:95], v[128:131], v[108:111], 0\n" \
"v_exp_f32 v66, v66\n" \
"v_exp_f32 v67, v67\n" \
"v_exp_f32 v68, v68\n" \
"v_exp_f32 v69, v69\n" \
"v_cvt_pk_bf16_f32 v96, v64, v65\n" \
"v_cvt_pk_bf16_f32 v97, v66, v67\n" \
"v_mfma_f32_32x32x8_bf16 v[80:95], v[132:133], v[122:123], v[80:95]\n" \
"v_exp_f32 v70, v70\n" \
"v_exp_f32 v71, v71\n" \
"v_exp_f32 v72, v72\n" \
"v_exp_f32 v73, v73\n" \
"v_cvt_pk_bf16_f32 v98, v68, v69\n" \
"v_cvt_pk_bf16_f32 v99, v70, v71\n" \
"v_mfma_f32_32x32x16_bf16 v[48:63], v[152:155], v[100:103], v[48:63]\n" \
"buffer_load_dwordx4 v[148:151], v156, s[56:59], s40 offen offset:1024 sc1\n" \
"buffer_load_dwordx4 v[152:155], v156, s[56:59], s40 offen offset:2048 sc1\n" \
"s_add_u32 s40, s40, 4096\n" \
"buffer_load_dwordx4 v[240:243], v156, s[56:59], s48 offen offset:1024 sc1\n" \
"buffer_load_dwordx4 v[244:247], v156, s[56:59], s49 offen offset:1024 sc1\n" \
"buffer_load_dwordx4 v156, s[56:59], s52 offen offset:3072 sc1 lds\n" \
"v_exp_f32 v74, v74\n" \
"v_exp_f32 v75, v75\n" \
"v_exp_f32 v76, v76\n" \
"v_exp_f32 v77, v77\n" \
"v_cvt_pk_bf16_f32 v100, v72, v73\n" \
"v_cvt_pk_bf16_f32 v101, v74, v75\n" \
"s_waitcnt vmcnt(11)\n" \
"v_mfma_f32_32x32x16_bf16 v[0:15], v[140:143], v[96:99], v[0:15]\n" \
"v_exp_f32 v78, v78\n" \
"v_exp_f32 v79, v79\n" \
"v_exp_f32 v80, v80\n" \
"v_exp_f32 v81, v81\n" \
"v_cvt_pk_bf16_f32 v102, v76, v77\n" \
"v_cvt_pk_bf16_f32 v103, v78, v79\n" \
"v_mfma_f32_32x32x16_bf16 v[64:79], v[128:131], v[112:115], 0\n" \
"v_exp_f32 v82, v82\n" \
"v_exp_f32 v83, v83\n" \
"v_exp_f32 v84, v84\n" \
"v_exp_f32 v85, v85\n" \
"v_cvt_pk_bf16_f32 v96, v80, v81\n" \
"v_cvt_pk_bf16_f32 v97, v82, v83\n" \
"v_mfma_f32_32x32x8_bf16 v[64:79], v[132:133], v[124:125], v[64:79]\n" \
"v_exp_f32 v86, v86\n" \
"v_exp_f32 v87, v87\n" \
"v_exp_f32 v88, v88\n" \
"v_exp_f32 v89, v89\n" \
"v_cvt_pk_bf16_f32 v98, v84, v85\n" \
"v_cvt_pk_bf16_f32 v99, v86, v87\n" \
"v_mfma_f32_32x32x16_bf16 v[0:15], v[144:147], v[100:103], v[0:15]\n" \
"v_exp_f32 v90, v90\n" \
"v_exp_f32 v91, v91\n" \
"v_exp_f32 v92, v92\n" \
"v_exp_f32 v93, v93\n" \
"v_cvt_pk_bf16_f32 v100, v88, v89\n" \
"v_cvt_pk_bf16_f32 v101, v90, v91\n" \
"v_mfma_f32_32x32x16_bf16 v[16:31], v[140:143], v[96:99], v[16:31]\n" \
"v_exp_f32 v94, v94\n" \
"v_exp_f32 v95, v95\n" \
"v_exp_f32 v64, v64\n" \
"v_exp_f32 v65, v65\n" \
"v_cvt_pk_bf16_f32 v102, v92, v93\n" \
"v_cvt_pk_bf16_f32 v103, v94, v95\n" \
"v_mfma_f32_32x32x16_bf16 v[80:95], v[128:131], v[116:119], 0\n" \
"v_exp_f32 v66, v66\n" \
"v_exp_f32 v67, v67\n" \
"v_exp_f32 v68, v68\n" \
"v_exp_f32 v69, v69\n" \
"v_cvt_pk_bf16_f32 v96, v64, v65\n" \
"v_cvt_pk_bf16_f32 v97, v66, v67\n" \
"v_mfma_f32_32x32x8_bf16 v[80:95], v[132:133], v[126:127], v[80:95]\n" \
"v_exp_f32 v70, v70\n" \
"v_exp_f32 v71, v71\n" \
"v_exp_f32 v72, v72\n" \
"v_exp_f32 v73, v73\n" \
"v_cvt_pk_bf16_f32 v98, v68, v69\n" \
"v_cvt_pk_bf16_f32 v99, v70, v71\n" \
"v_mfma_f32_32x32x16_bf16 v[16:31], v[144:147], v[100:103], v[16:31]\n" \
"v_exp_f32 v74, v74\n" \
"v_exp_f32 v75, v75\n" \
"v_exp_f32 v76, v76\n" \
"v_exp_f32 v77, v77\n" \
"v_cvt_pk_bf16_f32 v100, v72, v73\n" \
"v_cvt_pk_bf16_f32 v101, v74, v75\n" \
"v_mfma_f32_32x32x16_bf16 v[32:47], v[140:143], v[96:99], v[32:47]\n" \
"v_exp_f32 v78, v78\n" \
"v_exp_f32 v79, v79\n" \
"v_exp_f32 v80, v80\n" \
"v_exp_f32 v81, v81\n" \
"v_cvt_pk_bf16_f32 v102, v76, v77\n" \
"v_cvt_pk_bf16_f32 v103, v78, v79\n" \
"s_waitcnt vmcnt(6)\n" \
"v_mfma_f32_32x32x16_bf16 v[64:79], v[134:137], v[104:107], 0\n" \
"v_exp_f32 v82, v82\n" \
"v_exp_f32 v83, v83\n" \
"v_exp_f32 v84, v84\n" \
"v_exp_f32 v85, v85\n" \
"v_cvt_pk_bf16_f32 v96, v80, v81\n" \
"v_cvt_pk_bf16_f32 v97, v82, v83\n" \
"v_mfma_f32_32x32x8_bf16 v[64:79], v[138:139], v[120:121], v[64:79]\n" \
"buffer_load_dwordx4 v[128:131], v156, s[56:59], s76 offen sc1\n" \
"s_nop 0\n" \
"buffer_load_dwordx4 v[248:251], v156, s[56:59], s48 offen offset:2048 sc1\n" \
"v_exp_f32 v86, v86\n" \
"v_exp_f32 v87, v87\n" \
"v_exp_f32 v88, v88\n" \
"v_exp_f32 v89, v89\n" \
"v_cvt_pk_bf16_f32 v98, v84, v85\n" \
"v_cvt_pk_bf16_f32 v99, v86, v87\n" \
"v_mfma_f32_32x32x16_bf16 v[32:47], v[144:147], v[100:103], v[32:47]\n" \
"v_exp_f32 v90, v90\n" \
"v_exp_f32 v91, v91\n" \
"v_exp_f32 v92, v92\n" \
"v_exp_f32 v93, v93\n" \
"v_cvt_pk_bf16_f32 v100, v88, v89\n" \
"v_cvt_pk_bf16_f32 v101, v90, v91\n" \
"v_mfma_f32_32x32x16_bf16 v[48:63], v[140:143], v[96:99], v[48:63]\n" \
"v_exp_f32 v94, v94\n" \
"v_exp_f32 v95, v95\n" \
"v_exp_f32 v64, v64\n" \
"v_exp_f32 v65, v65\n" \
"v_cvt_pk_bf16_f32 v102, v92, v93\n" \
"v_cvt_pk_bf16_f32 v103, v94, v95\n" \
"v_mfma_f32_32x32x16_bf16 v[80:95], v[134:137], v[108:111], 0\n" \
"v_exp_f32 v66, v66\n" \
"v_exp_f32 v67, v67\n" \
"v_exp_f32 v68, v68\n" \
"v_exp_f32 v69, v69\n" \
"v_cvt_pk_bf16_f32 v96, v64, v65\n" \
"v_cvt_pk_bf16_f32 v97, v66, v67\n" \
"v_mfma_f32_32x32x8_bf16 v[80:95], v[138:139], v[122:123], v[80:95]\n" \
"v_exp_f32 v70, v70\n" \
"v_exp_f32 v71, v71\n" \
"v_exp_f32 v72, v72\n" \
"v_exp_f32 v73, v73\n" \
"v_cvt_pk_bf16_f32 v98, v68, v69\n" \
"v_cvt_pk_bf16_f32 v99, v70, v71\n" \
"v_mfma_f32_32x32x16_bf16 v[48:63], v[144:147], v[100:103], v[48:63]\n" \
"buffer_load_dwordx4 v[140:143], v163, s[80:83], s78 offen\n" \
"buffer_load_dwordx4 v[144:147], v164, s[80:83], s78 offen\n" \
"s_add_u32 s40, s40, 4096\n" \
"buffer_load_dwordx4 v[252:255], v156, s[56:59], s49 offen offset:2048 sc1\n" \
"s_add_u32 s52, s52, 4096\n" \
"s_add_u32 m0, m0, 4096\n" \
"s_nop 0\n" \
"buffer_load_dwordx4 v156, s[56:59], s52 offen offset:0 sc1 lds\n" \
"v_exp_f32 v74, v74\n" \
"v_exp_f32 v75, v75\n" \
"v_exp_f32 v76, v76\n" \
"v_exp_f32 v77, v77\n" \
"v_cvt_pk_bf16_f32 v100, v72, v73\n" \
"v_cvt_pk_bf16_f32 v101, v74, v75\n" \
"s_waitcnt vmcnt(9)\n" \
"v_mfma_f32_32x32x16_bf16 v[0:15], v[148:151], v[96:99], v[0:15]\n" \
"v_exp_f32 v78, v78\n" \
"v_exp_f32 v79, v79\n" \
"v_exp_f32 v80, v80\n" \
"v_exp_f32 v81, v81\n" \
"v_cvt_pk_bf16_f32 v102, v76, v77\n" \
"v_cvt_pk_bf16_f32 v103, v78, v79\n" \
"v_mfma_f32_32x32x16_bf16 v[64:79], v[134:137], v[112:115], 0\n" \
"v_exp_f32 v82, v82\n" \
"v_exp_f32 v83, v83\n" \
"v_exp_f32 v84, v84\n" \
"v_exp_f32 v85, v85\n" \
"v_cvt_pk_bf16_f32 v96, v80, v81\n" \
"v_cvt_pk_bf16_f32 v97, v82, v83\n" \
"v_mfma_f32_32x32x8_bf16 v[64:79], v[138:139], v[124:125], v[64:79]\n" \
"v_exp_f32 v86, v86\n" \
"v_exp_f32 v87, v87\n" \
"v_exp_f32 v88, v88\n" \
"v_exp_f32 v89, v89\n" \
"v_cvt_pk_bf16_f32 v98, v84, v85\n" \
"v_cvt_pk_bf16_f32 v99, v86, v87\n" \
"v_mfma_f32_32x32x16_bf16 v[0:15], v[152:155], v[100:103], v[0:15]\n" \
"v_exp_f32 v90, v90\n" \
"v_exp_f32 v91, v91\n" \
"v_exp_f32 v92, v92\n" \
"v_exp_f32 v93, v93\n" \
"v_cvt_pk_bf16_f32 v100, v88, v89\n" \
"v_cvt_pk_bf16_f32 v101, v90, v91\n" \
"v_mfma_f32_32x32x16_bf16 v[16:31], v[148:151], v[96:99], v[16:31]\n" \
"v_exp_f32 v94, v94\n" \
"v_exp_f32 v95, v95\n" \
"v_exp_f32 v64, v64\n" \
"v_exp_f32 v65, v65\n" \
"v_cvt_pk_bf16_f32 v102, v92, v93\n" \
"v_cvt_pk_bf16_f32 v103, v94, v95\n" \
"v_mfma_f32_32x32x16_bf16 v[80:95], v[134:137], v[116:119], 0\n" \
"v_exp_f32 v66, v66\n" \
"v_exp_f32 v67, v67\n" \
"v_exp_f32 v68, v68\n" \
"v_exp_f32 v69, v69\n" \
"v_cvt_pk_bf16_f32 v96, v64, v65\n" \
"v_cvt_pk_bf16_f32 v97, v66, v67\n" \
"v_mfma_f32_32x32x8_bf16 v[80:95], v[138:139], v[126:127], v[80:95]\n" \
"v_exp_f32 v70, v70\n" \
"v_exp_f32 v71, v71\n" \
"v_exp_f32 v72, v72\n" \
"v_exp_f32 v73, v73\n" \
"v_cvt_pk_bf16_f32 v98, v68, v69\n" \
"v_cvt_pk_bf16_f32 v99, v70, v71\n" \
"v_mfma_f32_32x32x16_bf16 v[16:31], v[152:155], v[100:103], v[16:31]\n" \
"v_exp_f32 v74, v74\n" \
"v_exp_f32 v75, v75\n" \
"v_exp_f32 v76, v76\n" \
"v_exp_f32 v77, v77\n" \
"v_cvt_pk_bf16_f32 v100, v72, v73\n" \
"v_cvt_pk_bf16_f32 v101, v74, v75\n" \
"v_mfma_f32_32x32x16_bf16 v[32:47], v[148:151], v[96:99], v[32:47]\n" \
"v_exp_f32 v78, v78\n" \
"v_exp_f32 v79, v79\n" \
"v_exp_f32 v80, v80\n" \
"v_exp_f32 v81, v81\n" \
"v_cvt_pk_bf16_f32 v102, v76, v77\n" \
"v_cvt_pk_bf16_f32 v103, v78, v79\n" \
"s_waitcnt vmcnt(5)\n" \
"v_mfma_f32_32x32x16_bf16 v[64:79], v[128:131], v[104:107], 0\n" \
"v_exp_f32 v82, v82\n" \
"v_exp_f32 v83, v83\n" \
"v_exp_f32 v84, v84\n" \
"v_exp_f32 v85, v85\n" \
"v_cvt_pk_bf16_f32 v96, v80, v81\n" \
"v_cvt_pk_bf16_f32 v97, v82, v83\n" \
"v_mfma_f32_32x32x8_bf16 v[64:79], v[132:133], v[120:121], v[64:79]\n" \
"buffer_load_dwordx4 v[134:137], v156, s[56:59], s77 offen sc1\n" \
"s_nop 0\n" \
"v_exp_f32 v86, v86\n" \
"v_exp_f32 v87, v87\n" \
"v_exp_f32 v88, v88\n" \
"v_exp_f32 v89, v89\n" \
"v_cvt_pk_bf16_f32 v98, v84, v85\n" \
"v_cvt_pk_bf16_f32 v99, v86, v87\n" \
"v_mfma_f32_32x32x16_bf16 v[32:47], v[152:155], v[100:103], v[32:47]\n" \
"v_exp_f32 v90, v90\n" \
"v_exp_f32 v91, v91\n" \
"v_exp_f32 v92, v92\n" \
"v_exp_f32 v93, v93\n" \
"v_cvt_pk_bf16_f32 v100, v88, v89\n" \
"v_cvt_pk_bf16_f32 v101, v90, v91\n" \
"v_mfma_f32_32x32x16_bf16 v[48:63], v[148:151], v[96:99], v[48:63]\n" \
"v_exp_f32 v94, v94\n" \
"v_exp_f32 v95, v95\n" \
"s_nop 1\n" \
"v_cvt_pk_bf16_f32 v102, v92, v93\n" \
"v_cvt_pk_bf16_f32 v103, v94, v95\n" \
"s_nop 1\n" \
"v_mfma_f32_32x32x16_bf16 v[48:63], v[152:155], v[100:103], v[48:63]\n" \
"s_setprio 0\n" \
"s_waitcnt vmcnt(0)\n" \
"s_mov_b32 m0, s70\n" \
"s_nop 7\n" \
"s_nop 7\n"

#define ATTN_PRE \
"buffer_load_dwordx4 v[128:131], v156, s[56:59], s40 offen sc1\n" \
"buffer_load_dwordx2 v[132:133], v156, s[56:59], s40 offen offset:3072 sc1\n" \
"buffer_load_dwordx4 v[140:143], v156, s[56:59], s40 offen offset:1024 sc1\n" \
"buffer_load_dwordx4 v[144:147], v156, s[56:59], s40 offen offset:2048 sc1\n" \
"s_add_u32 s40, s40, 4096\n" \
"buffer_load_dwordx4 v[134:137], v156, s[56:59], s40 offen sc1\n" \
"buffer_load_dwordx2 v[138:139], v156, s[56:59], s40 offen offset:3072 sc1\n"

#define A1_ASM \
"s_mov_b32 %[keep], m0\n" \
"v_lshlrev_b32 v224, 16, v176\n" \
"v_and_b32 v225, 0xffff0000, v176\n" \
"v_lshlrev_b32 v226, 16, v177\n" \
"v_and_b32 v227, 0xffff0000, v177\n" \
"v_pk_fma_f32 v[224:225], v[224:225], %[rc], %[rc] op_sel:[0,0,1] op_sel_hi:[1,0,1]\n" \
"v_pk_fma_f32 v[226:227], v[226:227], %[rc], %[rc] op_sel:[0,0,1] op_sel_hi:[1,0,1]\n" \
"v_cvt_pk_bf16_f32 v216, v224, v225\n" \
"v_cvt_pk_bf16_f32 v217, v226, v227\n" \
"v_lshlrev_b32 v224, 16, v178\n" \
"v_and_b32 v225, 0xffff0000, v178\n" \
"v_lshlrev_b32 v226, 16, v179\n" \
"v_and_b32 v227, 0xffff0000, v179\n" \
"v_pk_fma_f32 v[224:225], v[224:225], %[rc], %[rc] op_sel:[0,0,1] op_sel_hi:[1,0,1]\n" \
"v_pk_fma_f32 v[226:227], v[226:227], %[rc], %[rc] op_sel:[0,0,1] op_sel_hi:[1,0,1]\n" \
"v_cvt_pk_bf16_f32 v218, v224, v225\n" \
"v_cvt_pk_bf16_f32 v219, v226, v227\n" \
"ds_write_b128 %[xw], v[216:219] offset:0\n" \
"v_lshlrev_b32 v224, 16, v180\n" \
"v_and_b32 v225, 0xffff0000, v180\n" \
"v_lshlrev_b32 v226, 16, v181\n" \
"v_and_b32 v227, 0xffff0000, v181\n" \
"v_pk_fma_f32 v[224:225], v[224:225], %[rc], %[rc] op_sel:[0,0,1] op_sel_hi:[1,0,1]\n" \
"v_pk_fma_f32 v[226:227], v[226:227], %[rc], %[rc] op_sel:[0,0,1] op_sel_hi:[1,0,1]\n" \
"v_cvt_pk_bf16_f32 v220, v224, v225\n" \
"v_cvt_pk_bf16_f32 v221, v226, v227\n" \
"v_lshlrev_b32 v224, 16, v182\n" \
"v_and_b32 v225, 0xffff0000, v182\n" \
"v_lshlrev_b32 v226, 16, v183\n" \
"v_and_b32 v227, 0xffff0000, v183\n" \
"v_pk_fma_f32 v[224:225], v[224:225], %[rc], %[rc] op_sel:[0,0,1] op_sel_hi:[1,0,1]\n" \
"v_pk_fma_f32 v[226:227], v[226:227], %[rc], %[rc] op_sel:[0,0,1] op_sel_hi:[1,0,1]\n" \
"v_cvt_pk_bf16_f32 v222, v224, v225\n" \
"v_cvt_pk_bf16_f32 v223, v226, v227\n" \
"ds_write_b128 %[xw], v[220:223] offset:128\n" \
"s_add_u32 m0, %[sd], 24576\n" \
"v_lshl_add_u64 %[pa0], %[pa0], 0, %[st0]\n" \
"global_load_lds_dwordx4 %[pa0], off\n" \
"v_lshlrev_b32 v224, 16, v184\n" \
"v_and_b32 v225, 0xffff0000, v184\n" \
"v_lshlrev_b32 v226, 16, v185\n" \
"v_and_b32 v227, 0xffff0000, v185\n" \
"v_pk_fma_f32 v[224:225], v[224:225], %[rc], %[rc] op_sel:[0,0,1] op_sel_hi:[1,0,1]\n" \
"v_pk_fma_f32 v[226:227], v[226:227], %[rc], %[rc] op_sel:[0,0,1] op_sel_hi:[1,0,1]\n" \
"v_cvt_pk_bf16_f32 v216, v224, v225\n" \
"v_cvt_pk_bf16_f32 v217, v226, v227\n" \
"v_lshlrev_b32 v224, 16, v186\n" \
"v_and_b32 v225, 0xffff0000, v186\n" \
"v_lshlrev_b32 v226, 16, v187\n" \
"v_and_b32 v227, 0xffff0000, v187\n" \
"v_pk_fma_f32 v[224:225], v[224:225], %[rc], %[rc] op_sel:[0,0,1] op_sel_hi:[1,0,1]\n" \
"v_pk_fma_f32 v[226:227], v[226:227], %[rc], %[rc] op_sel:[0,0,1] op_sel_hi:[1,0,1]\n" \
"v_cvt_pk_bf16_f32 v218, v224, v225\n" \
"v_cvt_pk_bf16_f32 v219, v226, v227\n" \
"ds_write_b128 %[xw], v[216:219] offset:256\n" \
"s_add_u32 m0, %[sd], 25600\n" \
"v_lshl_add_u64 %[pa1], %[pa1], 0, %[st1]\n" \
"global_load_lds_dwordx4 %[pa1], off\n" \
"v_lshlrev_b32 v224, 16, v188\n" \
"v_and_b32 v225, 0xffff0000, v188\n" \
"v_lshlrev_b32 v226, 16, v189\n" \
"v_and_b32 v227, 0xffff0000, v189\n" \
"v_pk_fma_f32 v[224:225], v[224:225], %[rc], %[rc] op_sel:[0,0,1] op_sel_hi:[1,0,1]\n" \
"v_pk_fma_f32 v[226:227], v[226:227], %[rc], %[rc] op_sel:[0,0,1] op_sel_hi:[1,0,1]\n" \
"v_cvt_pk_bf16_f32 v220, v224, v225\n" \
"v_cvt_pk_bf16_f32 v221, v226, v227\n" \
"v_lshlrev_b32 v224, 16, v190\n" \
"v_and_b32 v225, 0xffff0000, v190\n" \
"v_lshlrev_b32 v226, 16, v191\n" \
"v_and_b32 v227, 0xffff0000, v191\n" \
"v_pk_fma_f32 v[224:225], v[224:225], %[rc], %[rc] op_sel:[0,0,1] op_sel_hi:[1,0,1]\n" \
"v_pk_fma_f32 v[226:227], v[226:227], %[rc], %[rc] op_sel:[0,0,1] op_sel_hi:[1,0,1]\n" \
"v_cvt_pk_bf16_f32 v222, v224, v225\n" \
"v_cvt_pk_bf16_f32 v223, v226, v227\n" \
"ds_write_b128 %[xw], v[220:223] offset:384\n" \
"s_add_u32 m0, %[sd], 26624\n" \
"v_lshl_add_u64 %[pa2], %[pa2], 0, %[st2]\n" \
"global_load_lds_dwordx4 %[pa2], off\n" \
"v_lshlrev_b32 v224, 16, v192\n" \
"v_and_b32 v225, 0xffff0000, v192\n" \
"v_lshlrev_b32 v226, 16, v193\n" \
"v_and_b32 v227, 0xffff0000, v193\n" \
"v_pk_fma_f32 v[224:225], v[224:225], %[rc], %[rc] op_sel:[0,0,1] op_sel_hi:[1,0,1]\n" \
"v_pk_fma_f32 v[226:227], v[226:227], %[rc], %[rc] op_sel:[0,0,1] op_sel_hi:[1,0,1]\n" \
"v_cvt_pk_bf16_f32 v216, v224, v225\n" \
"v_cvt_pk_bf16_f32 v217, v226, v227\n" \
"v_lshlrev_b32 v224, 16, v194\n" \
"v_and_b32 v225, 0xffff0000, v194\n" \
"v_lshlrev_b32 v226, 16, v195\n" \
"v_and_b32 v227, 0xffff0000, v195\n" \
"v_pk_fma_f32 v[224:225], v[224:225], %[rc], %[rc] op_sel:[0,0,1] op_sel_hi:[1,0,1]\n" \
"v_pk_fma_f32 v[226:227], v[226:227], %[rc], %[rc] op_sel:[0,0,1] op_sel_hi:[1,0,1]\n" \
"v_cvt_pk_bf16_f32 v218, v224, v225\n" \
"v_cvt_pk_bf16_f32 v219, v226, v227\n" \
"ds_write_b128 %[xw], v[216:219] offset:512\n" \
"s_add_u32 m0, %[sd], 49152\n" \
"v_lshl_add_u64 %[pa0], %[pa0], 0, %[st0]\n" \
"global_load_lds_dwordx4 %[pa0], off\n" \
"v_lshlrev_b32 v224, 16, v196\n" \
"v_and_b32 v225, 0xffff0000, v196\n" \
"v_lshlrev_b32 v226, 16, v197\n" \
"v_and_b32 v227, 0xffff0000, v197\n" \
"v_pk_fma_f32 v[224:225], v[224:225], %[rc], %[rc] op_sel:[0,0,1] op_sel_hi:[1,0,1]\n" \
"v_pk_fma_f32 v[226:227], v[226:227], %[rc], %[rc] op_sel:[0,0,1] op_sel_hi:[1,0,1]\n" \
"v_cvt_pk_bf16_f32 v220, v224, v225\n" \
"v_cvt_pk_bf16_f32 v221, v226, v227\n" \
"v_lshlrev_b32 v224, 16, v198\n" \
"v_and_b32 v225, 0xffff0000, v198\n" \
"v_lshlrev_b32 v226, 16, v199\n" \
"v_and_b32 v227, 0xffff0000, v199\n" \
"v_pk_fma_f32 v[224:225], v[224:225], %[rc], %[rc] op_sel:[0,0,1] op_sel_hi:[1,0,1]\n" \
"v_pk_fma_f32 v[226:227], v[226:227], %[rc], %[rc] op_sel:[0,0,1] op_sel_hi:[1,0,1]\n" \
"v_cvt_pk_bf16_f32 v222, v224, v225\n" \
"v_cvt_pk_bf16_f32 v223, v226, v227\n" \
"ds_write_b128 %[xw], v[220:223] offset:640\n" \
"s_add_u32 m0, %[sd], 50176\n" \
"v_lshl_add_u64 %[pa1], %[pa1], 0, %[st1]\n" \
"global_load_lds_dwordx4 %[pa1], off\n" \
"v_lshlrev_b32 v224, 16, v200\n" \
"v_and_b32 v225, 0xffff0000, v200\n" \
"v_lshlrev_b32 v226, 16, v201\n" \
"v_and_b32 v227, 0xffff0000, v201\n" \
"v_pk_fma_f32 v[224:225], v[224:225], %[rc], %[rc] op_sel:[0,0,1] op_sel_hi:[1,0,1]\n" \
"v_pk_fma_f32 v[226:227], v[226:227], %[rc], %[rc] op_sel:[0,0,1] op_sel_hi:[1,0,1]\n" \
"v_cvt_pk_bf16_f32 v216, v224, v225\n" \
"v_cvt_pk_bf16_f32 v217, v226, v227\n" \
"v_lshlrev_b32 v224, 16, v202\n" \
"v_and_b32 v225, 0xffff0000, v202\n" \
"v_lshlrev_b32 v226, 16, v203\n" \
"v_and_b32 v227, 0xffff0000, v203\n" \
"v_pk_fma_f32 v[224:225], v[224:225], %[rc], %[rc] op_sel:[0,0,1] op_sel_hi:[1,0,1]\n" \
"v_pk_fma_f32 v[226:227], v[226:227], %[rc], %[rc] op_sel:[0,0,1] op_sel_hi:[1,0,1]\n" \
"v_cvt_pk_bf16_f32 v218, v224, v225\n" \
"v_cvt_pk_bf16_f32 v219, v226, v227\n" \
"ds_write_b128 %[xw], v[216:219] offset:768\n" \
"s_add_u32 m0, %[sd], 51200\n" \
"v_lshl_add_u64 %[pa2], %[pa2], 0, %[st2]\n" \
"global_load_lds_dwordx4 %[pa2], off\n" \
"v_lshlrev_b32 v224, 16, v204\n" \
"v_and_b32 v225, 0xffff0000, v204\n" \
"v_lshlrev_b32 v226, 16, v205\n" \
"v_and_b32 v227, 0xffff0000, v205\n" \
"v_pk_fma_f32 v[224:225], v[224:225], %[rc], %[rc] op_sel:[0,0,1] op_sel_hi:[1,0,1]\n" \
"v_pk_fma_f32 v[226:227], v[226:227], %[rc], %[rc] op_sel:[0,0,1] op_sel_hi:[1,0,1]\n" \
"v_cvt_pk_bf16_f32 v220, v224, v225\n" \
"v_cvt_pk_bf16_f32 v221, v226, v227\n" \
"v_lshlrev_b32 v224, 16, v206\n" \
"v_and_b32 v225, 0xffff0000, v206\n" \
"v_lshlrev_b32 v226, 16, v207\n" \
"v_and_b32 v227, 0xffff0000, v207\n" \
"v_pk_fma_f32 v[224:225], v[224:225], %[rc], %[rc] op_sel:[0,0,1] op_sel_hi:[1,0,1]\n" \
"v_pk_fma_f32 v[226:227], v[226:227], %[rc], %[rc] op_sel:[0,0,1] op_sel_hi:[1,0,1]\n" \
"v_cvt_pk_bf16_f32 v222, v224, v225\n" \
"v_cvt_pk_bf16_f32 v223, v226, v227\n" \
"ds_write_b128 %[xw], v[220:223] offset:896\n" \
"v_lshlrev_b32 v224, 16, v208\n" \
"v_and_b32 v225, 0xffff0000, v208\n" \
"v_lshlrev_b32 v226, 16, v209\n" \
"v_and_b32 v227, 0xffff0000, v209\n" \
"v_pk_fma_f32 v[224:225], v[224:225], %[rc], %[rc] op_sel:[0,0,1] op_sel_hi:[1,0,1]\n" \
"v_pk_fma_f32 v[226:227], v[226:227], %[rc], %[rc] op_sel:[0,0,1] op_sel_hi:[1,0,1]\n" \
"v_cvt_pk_bf16_f32 v216, v224, v225\n" \
"v_cvt_pk_bf16_f32 v217, v226, v227\n" \
"v_lshlrev_b32 v224, 16, v210\n" \
"v_and_b32 v225, 0xffff0000, v210\n" \
"v_lshlrev_b32 v226, 16, v211\n" \
"v_and_b32 v227, 0xffff0000, v211\n" \
"v_pk_fma_f32 v[224:225], v[224:225], %[rc], %[rc] op_sel:[0,0,1] op_sel_hi:[1,0,1]\n" \
"v_pk_fma_f32 v[226:227], v[226:227], %[rc], %[rc] op_sel:[0,0,1] op_sel_hi:[1,0,1]\n" \
"v_cvt_pk_bf16_f32 v218, v224, v225\n" \
"v_cvt_pk_bf16_f32 v219, v226, v227\n" \
"ds_write_b128 %[xw], v[216:219] offset:1024\n" \
"v_lshlrev_b32 v224, 16, v212\n" \
"v_and_b32 v225, 0xffff0000, v212\n" \
"v_lshlrev_b32 v226, 16, v213\n" \
"v_and_b32 v227, 0xffff0000, v213\n" \
"v_pk_fma_f32 v[224:225], v[224:225], %[rc], %[rc] op_sel:[0,0,1] op_sel_hi:[1,0,1]\n" \
"v_pk_fma_f32 v[226:227], v[226:227], %[rc], %[rc] op_sel:[0,0,1] op_sel_hi:[1,0,1]\n" \
"v_cvt_pk_bf16_f32 v220, v224, v225\n" \
"v_cvt_pk_bf16_f32 v221, v226, v227\n" \
"v_lshlrev_b32 v224, 16, v214\n" \
"v_and_b32 v225, 0xffff0000, v214\n" \
"v_lshlrev_b32 v226, 16, v215\n" \
"v_and_b32 v227, 0xffff0000, v215\n" \
"v_pk_fma_f32 v[224:225], v[224:225], %[rc], %[rc] op_sel:[0,0,1] op_sel_hi:[1,0,1]\n" \
"v_pk_fma_f32 v[226:227], v[226:227], %[rc], %[rc] op_sel:[0,0,1] op_sel_hi:[1,0,1]\n" \
"v_cvt_pk_bf16_f32 v222, v224, v225\n" \
"v_cvt_pk_bf16_f32 v223, v226, v227\n" \
"ds_write_b128 %[xw], v[220:223] offset:1152\n" \
"s_waitcnt vmcnt(6) lgkmcnt(0)\n" \
"s_barrier\n" \
"ds_read_b64_tr_b16 v[64:65], %[va] offset:0\n" \
"ds_read_b64_tr_b16 v[66:67], %[va] offset:768\n" \
"ds_read_b128 v[68:71], %[vb] offset:0\n" \
"ds_read_b64_tr_b16 v[72:73], %[va] offset:3072\n" \
"ds_read_b64_tr_b16 v[74:75], %[va] offset:3840\n" \
"ds_read_b128 v[76:79], %[vb] offset:32\n" \
"ds_read_b64_tr_b16 v[80:81], %[va] offset:6144\n" \
"ds_read_b64_tr_b16 v[82:83], %[va] offset:6912\n" \
"ds_read_b128 v[84:87], %[vb] offset:64\n" \
"s_waitcnt lgkmcnt(6)\n" \
"v_mfma_f32_32x32x16_bf16 %[acc0], v[64:67], v[68:71], 0\n" \
"ds_read_b64_tr_b16 v[88:89], %[va] offset:9216\n" \
"ds_read_b64_tr_b16 v[90:91], %[va] offset:9984\n" \
"ds_read_b128 v[92:95], %[vb] offset:96\n" \
"s_waitcnt lgkmcnt(6)\n" \
"v_mfma_f32_32x32x16_bf16 %[acc0], v[72:75], v[76:79], %[acc0]\n" \
"ds_read_b64_tr_b16 v[64:65], %[va] offset:12288\n" \
"ds_read_b64_tr_b16 v[66:67], %[va] offset:13056\n" \
"ds_read_b128 v[68:71], %[vb] offset:128\n" \
"s_waitcnt lgkmcnt(6)\n" \
"v_mfma_f32_32x32x16_bf16 %[acc0], v[80:83], v[84:87], %[acc0]\n" \
"ds_read_b64_tr_b16 v[72:73], %[va] offset:15360\n" \
"ds_read_b64_tr_b16 v[74:75], %[va] offset:16128\n" \
"ds_read_b128 v[76:79], %[vb] offset:160\n" \
"s_waitcnt lgkmcnt(6)\n" \
"v_mfma_f32_32x32x16_bf16 %[acc0], v[88:91], v[92:95], %[acc0]\n" \
"ds_read_b64_tr_b16 v[80:81], %[va] offset:18432\n" \
"ds_read_b64_tr_b16 v[82:83], %[va] offset:19200\n" \
"ds_read_b128 v[84:87], %[vb] offset:192\n" \
"s_waitcnt lgkmcnt(6)\n" \
"v_mfma_f32_32x32x16_bf16 %[acc0], v[64:67], v[68:71], %[acc0]\n" \
"ds_read_b64_tr_b16 v[88:89], %[va] offset:21504\n" \
"ds_read_b64_tr_b16 v[90:91], %[va] offset:22272\n" \
"ds_read_b128 v[92:95], %[vb] offset:224\n" \
"s_waitcnt lgkmcnt(6)\n" \
"v_mfma_f32_32x32x16_bf16 %[acc0], v[72:75], v[76:79], %[acc0]\n" \
"s_waitcnt vmcnt(3) lgkmcnt(0)\n" \
"s_barrier\n" \
"ds_read_b64_tr_b16 v[64:65], %[va] offset:24576\n" \
"ds_read_b64_tr_b16 v[66:67], %[va] offset:25344\n" \
"ds_read_b128 v[68:71], %[vb] offset:256\n" \
"ds_read_b64_tr_b16 v[72:73], %[va] offset:27648\n" \
"ds_read_b64_tr_b16 v[74:75], %[va] offset:28416\n" \
"ds_read_b128 v[76:79], %[vb] offset:288\n" \
"v_mfma_f32_32x32x16_bf16 %[acc0], v[80:83], v[84:87], %[acc0]\n" \
"v_mfma_f32_32x32x16_bf16 %[acc0], v[88:91], v[92:95], %[acc0]\n" \
"ds_read_b64_tr_b16 v[80:81], %[va] offset:30720\n" \
"ds_read_b64_tr_b16 v[82:83], %[va] offset:31488\n" \
"ds_read_b128 v[84:87], %[vb] offset:320\n" \
"s_waitcnt lgkmcnt(6)\n" \
"v_mfma_f32_32x32x16_bf16 %[acc0], v[64:67], v[68:71], %[acc0]\n" \
"s_add_u32 m0, %[sd], 0\n" \
"v_lshl_add_u64 %[pa0], %[pa0], 0, %[st0]\n" \
"global_load_lds_dwordx4 %[pa0], off\n" \
"ds_read_b64_tr_b16 v[88:89], %[va] offset:33792\n" \
"ds_read_b64_tr_b16 v[90:91], %[va] offset:34560\n" \
"ds_read_b128 v[92:95], %[vb] offset:352\n" \
"s_waitcnt lgkmcnt(6)\n" \
"v_mfma_f32_32x32x16_bf16 %[acc0], v[72:75], v[76:79], %[acc0]\n" \
"ds_read_b64_tr_b16 v[64:65], %[va] offset:36864\n" \
"ds_read_b64_tr_b16 v[66:67], %[va] offset:37632\n" \
"ds_read_b128 v[68:71], %[vb] offset:384\n" \
"s_waitcnt lgkmcnt(6)\n" \
"v_mfma_f32_32x32x16_bf16 %[acc0], v[80:83], v[84:87], %[acc0]\n" \
"s_add_u32 m0, %[sd], 1024\n" \
"v_lshl_add_u64 %[pa1], %[pa1], 0, %[st1]\n" \
"global_load_lds_dwordx4 %[pa1], off\n" \
"ds_read_b64_tr_b16 v[72:73], %[va] offset:39936\n" \
"ds_read_b64_tr_b16 v[74:75], %[va] offset:40704\n" \
"ds_read_b128 v[76:79], %[vb] offset:416\n" \
"s_waitcnt lgkmcnt(6)\n" \
"v_mfma_f32_32x32x16_bf16 %[acc0], v[88:91], v[92:95], %[acc0]\n" \
"ds_read_b64_tr_b16 v[80:81], %[va] offset:43008\n" \
"ds_read_b64_tr_b16 v[82:83], %[va] offset:43776\n" \
"ds_read_b128 v[84:87], %[vb] offset:448\n" \
"s_waitcnt lgkmcnt(6)\n" \
"v_mfma_f32_32x32x16_bf16 %[acc0], v[64:67], v[68:71], %[acc0]\n" \
"s_add_u32 m0, %[sd], 2048\n" \
"v_lshl_add_u64 %[pa2], %[pa2], 0, %[st2]\n" \
"global_load_lds_dwordx4 %[pa2], off\n" \
"ds_read_b64_tr_b16 v[88:89], %[va] offset:46080\n" \
"ds_read_b64_tr_b16 v[90:91], %[va] offset:46848\n" \
"ds_read_b128 v[92:95], %[vb] offset:480\n" \
"s_waitcnt lgkmcnt(6)\n" \
"v_mfma_f32_32x32x16_bf16 %[acc0], v[72:75], v[76:79], %[acc0]\n" \
"s_waitcnt vmcnt(3) lgkmcnt(0)\n" \
"s_barrier\n" \
"ds_read_b64_tr_b16 v[64:65], %[va2] offset:0\n" \
"ds_read_b64_tr_b16 v[66:67], %[va2] offset:768\n" \
"ds_read_b128 v[68:71], %[vb] offset:512\n" \
"ds_read_b64_tr_b16 v[72:73], %[va2] offset:3072\n" \
"ds_read_b64_tr_b16 v[74:75], %[va2] offset:3840\n" \
"ds_read_b128 v[76:79], %[vb] offset:544\n" \
"v_mfma_f32_32x32x16_bf16 %[acc0], v[80:83], v[84:87], %[acc0]\n" \
"v_mfma_f32_32x32x16_bf16 %[acc0], v[88:91], v[92:95], %[acc0]\n" \
"ds_read_b64_tr_b16 v[80:81], %[va2] offset:6144\n" \
"ds_read_b64_tr_b16 v[82:83], %[va2] offset:6912\n" \
"ds_read_b128 v[84:87], %[vb] offset:576\n" \
"s_waitcnt lgkmcnt(6)\n" \
"v_mfma_f32_32x32x16_bf16 %[acc0], v[64:67], v[68:71], %[acc0]\n" \
"s_add_u32 m0, %[sd], 24576\n" \
"v_lshl_add_u64 %[pa0], %[pa0], 0, %[st0]\n" \
"global_load_lds_dwordx4 %[pa0], off\n" \
"ds_read_b64_tr_b16 v[88:89], %[va2] offset:9216\n" \
"ds_read_b64_tr_b16 v[90:91], %[va2] offset:9984\n" \
"ds_read_b128 v[92:95], %[vb] offset:608\n" \
"s_waitcnt lgkmcnt(6)\n" \
"v_mfma_f32_32x32x16_bf16 %[acc0], v[72:75], v[76:79], %[acc0]\n" \
"ds_read_b64_tr_b16 v[64:65], %[va2] offset:12288\n" \
"ds_read_b64_tr_b16 v[66:67], %[va2] offset:13056\n" \
"ds_read_b128 v[68:71], %[vb] offset:640\n" \
"s_waitcnt lgkmcnt(6)\n" \
"v_mfma_f32_32x32x16_bf16 %[acc0], v[80:83], v[84:87], %[acc0]\n" \
"s_add_u32 m0, %[sd], 25600\n" \
"v_lshl_add_u64 %[pa1], %[pa1], 0, %[st1]\n" \
"global_load_lds_dwordx4 %[pa1], off\n" \
"ds_read_b64_tr_b16 v[72:73], %[va2] offset:15360\n" \
"ds_read_b64_tr_b16 v[74:75], %[va2] offset:16128\n" \
"ds_read_b128 v[76:79], %[vb] offset:672\n" \
"s_waitcnt lgkmcnt(6)\n" \
"v_mfma_f32_32x32x16_bf16 %[acc0], v[88:91], v[92:95], %[acc0]\n" \
"ds_read_b64_tr_b16 v[80:81], %[va2] offset:18432\n" \
"ds_read_b64_tr_b16 v[82:83], %[va2] offset:19200\n" \
"ds_read_b128 v[84:87], %[vb] offset:704\n" \
"s_waitcnt lgkmcnt(6)\n" \
"v_mfma_f32_32x32x16_bf16 %[acc0], v[64:67], v[68:71], %[acc0]\n" \
"s_add_u32 m0, %[sd], 26624\n" \
"v_lshl_add_u64 %[pa2], %[pa2], 0, %[st2]\n" \
"global_load_lds_dwordx4 %[pa2], off\n" \
"ds_read_b64_tr_b16 v[88:89], %[va2] offset:21504\n" \
"ds_read_b64_tr_b16 v[90:91], %[va2] offset:22272\n" \
"ds_read_b128 v[92:95], %[vb] offset:736\n" \
"s_waitcnt lgkmcnt(6)\n" \
"v_mfma_f32_32x32x16_bf16 %[acc0], v[72:75], v[76:79], %[acc0]\n" \
"s_waitcnt vmcnt(3) lgkmcnt(0)\n" \
"s_barrier\n" \
"ds_read_b64_tr_b16 v[64:65], %[va] offset:0\n" \
"ds_read_b64_tr_b16 v[66:67], %[va] offset:768\n" \
"ds_read_b128 v[68:71], %[vb] offset:768\n" \
"ds_read_b64_tr_b16 v[72:73], %[va] offset:3072\n" \
"ds_read_b64_tr_b16 v[74:75], %[va] offset:3840\n" \
"ds_read_b128 v[76:79], %[vb] offset:800\n" \
"v_mfma_f32_32x32x16_bf16 %[acc0], v[80:83], v[84:87], %[acc0]\n" \
"v_mfma_f32_32x32x16_bf16 %[acc0], v[88:91], v[92:95], %[acc0]\n" \
"ds_read_b64_tr_b16 v[80:81], %[va] offset:6144\n" \
"ds_read_b64_tr_b16 v[82:83], %[va] offset:6912\n" \
"ds_read_b128 v[84:87], %[vb] offset:832\n" \
"s_waitcnt lgkmcnt(6)\n" \
"v_mfma_f32_32x32x16_bf16 %[acc0], v[64:67], v[68:71], %[acc0]\n" \
"s_add_u32 m0, %[sd], 49152\n" \
"s_mov_b32 %[sf], 0\n" \
"buffer_load_dwordx4 %[of0], %[w1r], %[sf] offen lds\n" \
"ds_read_b64_tr_b16 v[88:89], %[va] offset:9216\n" \
"ds_read_b64_tr_b16 v[90:91], %[va] offset:9984\n" \
"ds_read_b128 v[92:95], %[vb] offset:864\n" \
"s_waitcnt lgkmcnt(6)\n" \
"v_mfma_f32_32x32x16_bf16 %[acc0], v[72:75], v[76:79], %[acc0]\n" \
"ds_read_b64_tr_b16 v[64:65], %[va] offset:12288\n" \
"ds_read_b64_tr_b16 v[66:67], %[va] offset:13056\n" \
"ds_read_b128 v[68:71], %[vb] offset:896\n" \
"s_waitcnt lgkmcnt(6)\n" \
"v_mfma_f32_32x32x16_bf16 %[acc0], v[80:83], v[84:87], %[acc0]\n" \
"s_add_u32 m0, %[sd], 50176\n" \
"s_nop 0\n" \
"buffer_load_dwordx4 %[of1], %[w1r], %[sf] offen lds\n" \
"ds_read_b64_tr_b16 v[72:73], %[va] offset:15360\n" \
"ds_read_b64_tr_b16 v[74:75], %[va] offset:16128\n" \
"ds_read_b128 v[76:79], %[vb] offset:928\n" \
"s_waitcnt lgkmcnt(6)\n" \
"v_mfma_f32_32x32x16_bf16 %[acc0], v[88:91], v[92:95], %[acc0]\n" \
"ds_read_b64_tr_b16 v[80:81], %[va] offset:18432\n" \
"ds_read_b64_tr_b16 v[82:83], %[va] offset:19200\n" \
"ds_read_b128 v[84:87], %[vb] offset:960\n" \
"s_waitcnt lgkmcnt(6)\n" \
"v_mfma_f32_32x32x16_bf16 %[acc0], v[64:67], v[68:71], %[acc0]\n" \
"s_add_u32 m0, %[sd], 51200\n" \
"s_nop 0\n" \
"buffer_load_dwordx4 %[of2], %[w1r], %[sf] offen lds\n" \
"ds_read_b64_tr_b16 v[88:89], %[va] offset:21504\n" \
"ds_read_b64_tr_b16 v[90:91], %[va] offset:22272\n" \
"ds_read_b128 v[92:95], %[vb] offset:992\n" \
"s_waitcnt lgkmcnt(6)\n" \
"v_mfma_f32_32x32x16_bf16 %[acc0], v[72:75], v[76:79], %[acc0]\n" \
"s_waitcnt vmcnt(3) lgkmcnt(0)\n" \
"s_barrier\n" \
"ds_read_b64_tr_b16 v[64:65], %[va] offset:24576\n" \
"ds_read_b64_tr_b16 v[66:67], %[va] offset:25344\n" \
"ds_read_b128 v[68:71], %[vb] offset:1024\n" \
"ds_read_b64_tr_b16 v[72:73], %[va] offset:27648\n" \
"ds_read_b64_tr_b16 v[74:75], %[va] offset:28416\n" \
"ds_read_b128 v[76:79], %[vb] offset:1056\n" \
"v_mfma_f32_32x32x16_bf16 %[acc0], v[80:83], v[84:87], %[acc0]\n" \
"v_mfma_f32_32x32x16_bf16 %[acc0], v[88:91], v[92:95], %[acc0]\n" \
"ds_read_b64_tr_b16 v[80:81], %[va] offset:30720\n" \
"ds_read_b64_tr_b16 v[82:83], %[va] offset:31488\n" \
"ds_read_b128 v[84:87], %[vb] offset:1088\n" \
"s_waitcnt lgkmcnt(6)\n" \
"v_mfma_f32_32x32x16_bf16 %[acc0], v[64:67], v[68:71], %[acc0]\n" \
"s_add_u32 m0, %[sd], 0\n" \
"s_mov_b32 %[sf], 196608\n" \
"buffer_load_dwordx4 %[of0], %[w1r], %[sf] offen lds\n" \
"ds_read_b64_tr_b16 v[88:89], %[va] offset:33792\n" \
"ds_read_b64_tr_b16 v[90:91], %[va] offset:34560\n" \
"ds_read_b128 v[92:95], %[vb] offset:1120\n" \
"s_waitcnt lgkmcnt(6)\n" \
"v_mfma_f32_32x32x16_bf16 %[acc0], v[72:75], v[76:79], %[acc0]\n" \
"ds_read_b64_tr_b16 v[64:65], %[va] offset:36864\n" \
"ds_read_b64_tr_b16 v[66:67], %[va] offset:37632\n" \
"ds_read_b128 v[68:71], %[vb] offset:1152\n" \
"s_waitcnt lgkmcnt(6)\n" \
"v_mfma_f32_32x32x16_bf16 %[acc0], v[80:83], v[84:87], %[acc0]\n" \
"s_add_u32 m0, %[sd], 1024\n" \
"s_nop 0\n" \
"buffer_load_dwordx4 %[of1], %[w1r], %[sf] offen lds\n" \
"ds_read_b64_tr_b16 v[72:73], %[va] offset:39936\n" \
"ds_read_b64_tr_b16 v[74:75], %[va] offset:40704\n" \
"ds_read_b128 v[76:79], %[vb] offset:1184\n" \
"s_waitcnt lgkmcnt(6)\n" \
"v_mfma_f32_32x32x16_bf16 %[acc0], v[88:91], v[92:95], %[acc0]\n" \
"ds_read_b64_tr_b16 v[80:81], %[va] offset:43008\n" \
"ds_read_b64_tr_b16 v[82:83], %[va] offset:43776\n" \
"ds_read_b128 v[84:87], %[vb] offset:1216\n" \
"s_waitcnt lgkmcnt(6)\n" \
"v_mfma_f32_32x32x16_bf16 %[acc0], v[64:67], v[68:71], %[acc0]\n" \
"s_add_u32 m0, %[sd], 2048\n" \
"s_nop 0\n" \
"buffer_load_dwordx4 %[of2], %[w1r], %[sf] offen lds\n" \
"ds_read_b64_tr_b16 v[88:89], %[va] offset:46080\n" \
"ds_read_b64_tr_b16 v[90:91], %[va] offset:46848\n" \
"ds_read_b128 v[92:95], %[vb] offset:1248\n" \
"s_waitcnt lgkmcnt(6)\n" \
"v_mfma_f32_32x32x16_bf16 %[acc0], v[72:75], v[76:79], %[acc0]\n" \
"s_waitcnt lgkmcnt(0)\n" \
"v_mfma_f32_32x32x16_bf16 %[acc0], v[80:83], v[84:87], %[acc0]\n" \
"v_mfma_f32_32x32x16_bf16 %[acc0], v[88:91], v[92:95], %[acc0]\n" \
"s_mov_b32 m0, %[keep]\n" \
"s_nop 7\n" \
"s_nop 7\n"

#define A2_ASM \
"s_mov_b32 %[keep], m0\n" \
"s_cmp_ge_u32 %[wid], 4\n" \
"s_cbranch_scc1 30f\n" \
"s_waitcnt vmcnt(3) lgkmcnt(0)\n" \
"s_barrier\n" \
"ds_read_b64_tr_b16 v[176:177], %[va2] offset:0\n" \
"ds_read_b64_tr_b16 v[178:179], %[va2] offset:1536\n" \
"ds_read_b128 v[180:183], %[vb] offset:0\n" \
"ds_read_b128 v[184:187], %[vb] offset:41472\n" \
"ds_read_b64_tr_b16 v[188:189], %[va2] offset:6144\n" \
"ds_read_b64_tr_b16 v[190:191], %[va2] offset:7680\n" \
"ds_read_b128 v[192:195], %[vb] offset:32\n" \
"ds_read_b128 v[196:199], %[vb] offset:41504\n" \
"s_add_u32 m0, %[sd], 24576\n" \
"s_mov_b32 %[sf], 393216\n" \
"buffer_load_dwordx4 %[of0], %[w1r], %[sf] offen lds\n" \
"ds_read_b64_tr_b16 v[200:201], %[va2] offset:12288\n" \
"ds_read_b64_tr_b16 v[202:203], %[va2] offset:13824\n" \
"ds_read_b128 v[204:207], %[vb] offset:64\n" \
"ds_read_b128 v[208:211], %[vb] offset:41536\n" \
"s_waitcnt lgkmcnt(8)\n" \
"v_mfma_f32_32x32x16_bf16 %[acc0], v[176:179], v[180:183], %[bias]\n" \
"v_mfma_f32_32x32x16_bf16 %[acc1], v[176:179], v[184:187], %[bias]\n" \
"s_add_u32 m0, %[sd], 25600\n" \
"s_nop 0\n" \
"buffer_load_dwordx4 %[of1], %[w1r], %[sf] offen lds\n" \
"ds_read_b64_tr_b16 v[212:213], %[va2] offset:18432\n" \
"ds_read_b64_tr_b16 v[214:215], %[va2] offset:19968\n" \
"ds_read_b128 v[216:219], %[vb] offset:96\n" \
"ds_read_b128 v[220:223], %[vb] offset:41568\n" \
"s_waitcnt lgkmcnt(8)\n" \
"v_mfma_f32_32x32x16_bf16 %[acc0], v[188:191], v[192:195], %[acc0]\n" \
"v_mfma_f32_32x32x16_bf16 %[acc1], v[188:191], v[196:199], %[acc1]\n" \
"s_add_u32 m0, %[sd], 26624\n" \
"s_nop 0\n" \
"buffer_load_dwordx4 %[of2], %[w1r], %[sf] offen lds\n" \
"s_waitcnt vmcnt(3) lgkmcnt(0)\n" \
"s_barrier\n" \
"s_cmp_lg_u32 %[wid], 0\n" \
"s_cbranch_scc1 101f\n" \
"s_mov_b64 exec, 0xff\n" \
"buffer_store_dwordx2 %[tagv], %[fst], s[56:59], 0 offen sc1\n" \
"s_mov_b64 exec, -1\n" \
"101:\n" \
"ds_read_b64_tr_b16 v[176:177], %[va] offset:0\n" \
"ds_read_b64_tr_b16 v[178:179], %[va] offset:1536\n" \
"ds_read_b128 v[180:183], %[vb] offset:128\n" \
"ds_read_b128 v[184:187], %[vb] offset:41600\n" \
"ds_read_b64_tr_b16 v[188:189], %[va] offset:6144\n" \
"ds_read_b64_tr_b16 v[190:191], %[va] offset:7680\n" \
"ds_read_b128 v[192:195], %[vb] offset:160\n" \
"ds_read_b128 v[196:199], %[vb] offset:41632\n" \
"v_mfma_f32_32x32x16_bf16 %[acc0], v[200:203], v[204:207], %[acc0]\n" \
"v_mfma_f32_32x32x16_bf16 %[acc1], v[200:203], v[208:211], %[acc1]\n" \
"v_mfma_f32_32x32x16_bf16 %[acc0], v[212:215], v[216:219], %[acc0]\n" \
"v_mfma_f32_32x32x16_bf16 %[acc1], v[212:215], v[220:223], %[acc1]\n" \
"s_add_u32 m0, %[sd], 49152\n" \
"s_mov_b32 %[sf], 589824\n" \
"buffer_load_dwordx4 %[of0], %[w1r], %[sf] offen lds\n" \
"ds_read_b64_tr_b16 v[200:201], %[va] offset:12288\n" \
"ds_read_b64_tr_b16 v[202:203], %[va] offset:13824\n" \
"ds_read_b128 v[204:207], %[vb] offset:192\n" \
"ds_read_b128 v[208:211], %[vb] offset:41664\n" \
"s_waitcnt lgkmcnt(8)\n" \
"v_mfma_f32_32x32x16_bf16 %[acc0], v[176:179], v[180:183], %[acc0]\n" \
"v_mfma_f32_32x32x16_bf16 %[acc1], v[176:179], v[184:187], %[acc1]\n" \
"s_add_u32 m0, %[sd], 50176\n" \
"s_nop 0\n" \
"buffer_load_dwordx4 %[of1], %[w1r], %[sf] offen lds\n" \
"ds_read_b64_tr_b16 v[212:213], %[va] offset:18432\n" \
"ds_read_b64_tr_b16 v[214:215], %[va] offset:19968\n" \
"ds_read_b128 v[216:219], %[vb] offset:224\n" \
"ds_read_b128 v[220:223], %[vb] offset:41696\n" \
"s_waitcnt lgkmcnt(8)\n" \
"v_mfma_f32_32x32x16_bf16 %[acc0], v[188:191], v[192:195], %[acc0]\n" \
"v_mfma_f32_32x32x16_bf16 %[acc1], v[188:191], v[196:199], %[acc1]\n" \
"s_add_u32 m0, %[sd], 51200\n" \
"s_nop 0\n" \
"buffer_load_dwordx4 %[of2], %[w1r], %[sf] offen lds\n" \
"s_waitcnt vmcnt(3) lgkmcnt(0)\n" \
"s_barrier\n" \
"ds_read_b64_tr_b16 v[176:177], %[va] offset:24576\n" \
"ds_read_b64_tr_b16 v[178:179], %[va] offset:26112\n" \
"ds_read_b128 v[180:183], %[vb] offset:256\n" \
"ds_read_b128 v[184:187], %[vb] offset:41728\n" \
"ds_read_b64_tr_b16 v[188:189], %[va] offset:30720\n" \
"ds_read_b64_tr_b16 v[190:191], %[va] offset:32256\n" \
"ds_read_b128 v[192:195], %[vb] offset:288\n" \
"ds_read_b128 v[196:199], %[vb] offset:41760\n" \
"v_mfma_f32_32x32x16_bf16 %[acc0], v[200:203], v[204:207], %[acc0]\n" \
"v_mfma_f32_32x32x16_bf16 %[acc1], v[200:203], v[208:211], %[acc1]\n" \
"v_mfma_f32_32x32x16_bf16 %[acc0], v[212:215], v[216:219], %[acc0]\n" \
"v_mfma_f32_32x32x16_bf16 %[acc1], v[212:215], v[220:223], %[acc1]\n" \
"s_add_u32 m0, %[sd], 0\n" \
"s_mov_b32 %[sf], 786432\n" \
"buffer_load_dwordx4 %[of0], %[w1r], %[sf] offen lds\n" \
"ds_read_b64_tr_b16 v[200:201], %[va] offset:36864\n" \
"ds_read_b64_tr_b16 v[202:203], %[va] offset:38400\n" \
"ds_read_b128 v[204:207], %[vb] offset:320\n" \
"ds_read_b128 v[208:211], %[vb] offset:41792\n" \
"s_waitcnt lgkmcnt(8)\n" \
"v_mfma_f32_32x32x16_bf16 %[acc0], v[176:179], v[180:183], %[acc0]\n" \
"v_mfma_f32_32x32x16_bf16 %[acc1], v[176:179], v[184:187], %[acc1]\n" \
"s_add_u32 m0, %[sd], 1024\n" \
"s_nop 0\n" \
"buffer_load_dwordx4 %[of1], %[w1r], %[sf] offen lds\n" \
"ds_read_b64_tr_b16 v[212:213], %[va] offset:43008\n" \
"ds_read_b64_tr_b16 v[214:215], %[va] offset:44544\n" \
"ds_read_b128 v[216:219], %[vb] offset:352\n" \
"ds_read_b128 v[220:223], %[vb] offset:41824\n" \
"s_waitcnt lgkmcnt(8)\n" \
"v_mfma_f32_32x32x16_bf16 %[acc0], v[188:191], v[192:195], %[acc0]\n" \
"v_mfma_f32_32x32x16_bf16 %[acc1], v[188:191], v[196:199], %[acc1]\n" \
"s_add_u32 m0, %[sd], 2048\n" \
"s_nop 0\n" \
"buffer_load_dwordx4 %[of2], %[w1r], %[sf] offen lds\n" \
"s_waitcnt vmcnt(3) lgkmcnt(0)\n" \
"s_barrier\n" \
"ds_read_b64_tr_b16 v[176:177], %[va2] offset:0\n" \
"ds_read_b64_tr_b16 v[178:179], %[va2] offset:1536\n" \
"ds_read_b128 v[180:183], %[vb] offset:384\n" \
"ds_read_b128 v[184:187], %[vb] offset:41856\n" \
"ds_read_b64_tr_b16 v[188:189], %[va2] offset:6144\n" \
"ds_read_b64_tr_b16 v[190:191], %[va2] offset:7680\n" \
"ds_read_b128 v[192:195], %[vb] offset:416\n" \
"ds_read_b128 v[196:199], %[vb] offset:41888\n" \
"v_mfma_f32_32x32x16_bf16 %[acc0], v[200:203], v[204:207], %[acc0]\n" \
"v_mfma_f32_32x32x16_bf16 %[acc1], v[200:203], v[208:211], %[acc1]\n" \
"v_mfma_f32_32x32x16_bf16 %[acc0], v[212:215], v[216:219], %[acc0]\n" \
"v_mfma_f32_32x32x16_bf16 %[acc1], v[212:215], v[220:223], %[acc1]\n" \
"s_add_u32 m0, %[sd], 24576\n" \
"s_mov_b32 %[sf], 983040\n" \
"buffer_load_dwordx4 %[of0], %[w1r], %[sf] offen lds\n" \
"ds_read_b64_tr_b16 v[200:201], %[va2] offset:12288\n" \
"ds_read_b64_tr_b16 v[202:203], %[va2] offset:13824\n" \
"ds_read_b128 v[204:207], %[vb] offset:448\n" \
"ds_read_b128 v[208:211], %[vb] offset:41920\n" \
"s_waitcnt lgkmcnt(8)\n" \
"v_mfma_f32_32x32x16_bf16 %[acc0], v[176:179], v[180:183], %[acc0]\n" \
"v_mfma_f32_32x32x16_bf16 %[acc1], v[176:179], v[184:187], %[acc1]\n" \
"s_add_u32 m0, %[sd], 25600\n" \
"s_nop 0\n" \
"buffer_load_dwordx4 %[of1], %[w1r], %[sf] offen lds\n" \
"ds_read_b64_tr_b16 v[212:213], %[va2] offset:18432\n" \
"ds_read_b64_tr_b16 v[214:215], %[va2] offset:19968\n" \
"ds_read_b128 v[216:219], %[vb] offset:480\n" \
"ds_read_b128 v[220:223], %[vb] offset:41952\n" \
"s_waitcnt lgkmcnt(8)\n" \
"v_mfma_f32_32x32x16_bf16 %[acc0], v[188:191], v[192:195], %[acc0]\n" \
"v_mfma_f32_32x32x16_bf16 %[acc1], v[188:191], v[196:199], %[acc1]\n" \
"s_add_u32 m0, %[sd], 26624\n" \
"s_nop 0\n" \
"buffer_load_dwordx4 %[of2], %[w1r], %[sf] offen lds\n" \
"s_waitcnt vmcnt(3) lgkmcnt(0)\n" \
"s_barrier\n" \
"ds_read_b64_tr_b16 v[176:177], %[va] offset:0\n" \
"ds_read_b64_tr_b16 v[178:179], %[va] offset:1536\n" \
"ds_read_b128 v[180:183], %[vb] offset:512\n" \
"ds_read_b128 v[184:187], %[vb] offset:41984\n" \
"ds_read_b64_tr_b16 v[188:189], %[va] offset:6144\n" \
"ds_read_b64_tr_b16 v[190:191], %[va] offset:7680\n" \
"ds_read_b128 v[192:195], %[vb] offset:544\n" \
"ds_read_b128 v[196:199], %[vb] offset:42016\n" \
"v_mfma_f32_32x32x16_bf16 %[acc0], v[200:203], v[204:207], %[acc0]\n" \
"v_mfma_f32_32x32x16_bf16 %[acc1], v[200:203], v[208:211], %[acc1]\n" \
"v_mfma_f32_32x32x16_bf16 %[acc0], v[212:215], v[216:219], %[acc0]\n" \
"v_mfma_f32_32x32x16_bf16 %[acc1], v[212:215], v[220:223], %[acc1]\n" \
"s_add_u32 m0, %[sd], 49152\n" \
"s_mov_b32 %[sf], 1179648\n" \
"buffer_load_dwordx4 %[of0], %[w1r], %[sf] offen lds\n" \
"ds_read_b64_tr_b16 v[200:201], %[va] offset:12288\n" \
"ds_read_b64_tr_b16 v[202:203], %[va] offset:13824\n" \
"ds_read_b128 v[204:207], %[vb] offset:576\n" \
"ds_read_b128 v[208:211], %[vb] offset:42048\n" \
"s_waitcnt lgkmcnt(8)\n" \
"v_mfma_f32_32x32x16_bf16 %[acc0], v[176:179], v[180:183], %[acc0]\n" \
"v_mfma_f32_32x32x16_bf16 %[acc1], v[176:179], v[184:187], %[acc1]\n" \
"s_add_u32 m0, %[sd], 50176\n" \
"s_nop 0\n" \
"buffer_load_dwordx4 %[of1], %[w1r], %[sf] offen lds\n" \
"ds_read_b64_tr_b16 v[212:213], %[va] offset:18432\n" \
"ds_read_b64_tr_b16 v[214:215], %[va] offset:19968\n" \
"ds_read_b128 v[216:219], %[vb] offset:608\n" \
"ds_read_b128 v[220:223], %[vb] offset:42080\n" \
"s_waitcnt lgkmcnt(8)\n" \
"v_mfma_f32_32x32x16_bf16 %[acc0], v[188:191], v[192:195], %[acc0]\n" \
"v_mfma_f32_32x32x16_bf16 %[acc1], v[188:191], v[196:199], %[acc1]\n" \
"s_add_u32 m0, %[sd], 51200\n" \
"s_nop 0\n" \
"buffer_load_dwordx4 %[of2], %[w1r], %[sf] offen lds\n" \
"s_waitcnt vmcnt(3) lgkmcnt(0)\n" \
"s_barrier\n" \
"ds_read_b64_tr_b16 v[176:177], %[va] offset:24576\n" \
"ds_read_b64_tr_b16 v[178:179], %[va] offset:26112\n" \
"ds_read_b128 v[180:183], %[vb] offset:640\n" \
"ds_read_b128 v[184:187], %[vb] offset:42112\n" \
"ds_read_b64_tr_b16 v[188:189], %[va] offset:30720\n" \
"ds_read_b64_tr_b16 v[190:191], %[va] offset:32256\n" \
"ds_read_b128 v[192:195], %[vb] offset:672\n" \
"ds_read_b128 v[196:199], %[vb] offset:42144\n" \
"buffer_load_dwordx2 v[148:149], %[fld], s[56:59], 0 offen sc1\n" \
"v_mfma_f32_32x32x16_bf16 %[acc0], v[200:203], v[204:207], %[acc0]\n" \
"v_mfma_f32_32x32x16_bf16 %[acc1], v[200:203], v[208:211], %[acc1]\n" \
"v_mfma_f32_32x32x16_bf16 %[acc0], v[212:215], v[216:219], %[acc0]\n" \
"v_mfma_f32_32x32x16_bf16 %[acc1], v[212:215], v[220:223], %[acc1]\n" \
"s_add_u32 m0, %[sd], 0\n" \
"s_mov_b32 %[sf], 1376256\n" \
"buffer_load_dwordx4 %[of0], %[w1r], %[sf] offen lds\n" \
"ds_read_b64_tr_b16 v[200:201], %[va] offset:36864\n" \
"ds_read_b64_tr_b16 v[202:203], %[va] offset:38400\n" \
"ds_read_b128 v[204:207], %[vb] offset:704\n" \
"ds_read_b128 v[208:211], %[vb] offset:42176\n" \
"s_waitcnt lgkmcnt(8)\n" \
"v_mfma_f32_32x32x16_bf16 %[acc0], v[176:179], v[180:183], %[acc0]\n" \
"v_mfma_f32_32x32x16_bf16 %[acc1], v[176:179], v[184:187], %[acc1]\n" \
"s_add_u32 m0, %[sd], 1024\n" \
"s_nop 0\n" \
"buffer_load_dwordx4 %[of1], %[w1r], %[sf] offen lds\n" \
"ds_read_b64_tr_b16 v[212:213], %[va] offset:43008\n" \
"ds_read_b64_tr_b16 v[214:215], %[va] offset:44544\n" \
"ds_read_b128 v[216:219], %[vb] offset:736\n" \
"ds_read_b128 v[220:223], %[vb] offset:42208\n" \
"s_waitcnt lgkmcnt(8)\n" \
"v_mfma_f32_32x32x16_bf16 %[acc0], v[188:191], v[192:195], %[acc0]\n" \
"v_mfma_f32_32x32x16_bf16 %[acc1], v[188:191], v[196:199], %[acc1]\n" \
"s_add_u32 m0, %[sd], 2048\n" \
"s_nop 0\n" \
"buffer_load_dwordx4 %[of2], %[w1r], %[sf] offen lds\n" \
"s_waitcnt vmcnt(4) lgkmcnt(0)\n" \
"s_barrier\n" \
"ds_read_b64_tr_b16 v[176:177], %[va2] offset:0\n" \
"ds_read_b64_tr_b16 v[178:179], %[va2] offset:1536\n" \
"ds_read_b128 v[180:183], %[vb] offset:768\n" \
"ds_read_b128 v[184:187], %[vb] offset:42240\n" \
"ds_read_b64_tr_b16 v[188:189], %[va2] offset:6144\n" \
"ds_read_b64_tr_b16 v[190:191], %[va2] offset:7680\n" \
"ds_read_b128 v[192:195], %[vb] offset:800\n" \
"ds_read_b128 v[196:199], %[vb] offset:42272\n" \
"v_mfma_f32_32x32x16_bf16 %[acc0], v[200:203], v[204:207], %[acc0]\n" \
"v_mfma_f32_32x32x16_bf16 %[acc1], v[200:203], v[208:211], %[acc1]\n" \
"v_mfma_f32_32x32x16_bf16 %[acc0], v[212:215], v[216:219], %[acc0]\n" \
"v_mfma_f32_32x32x16_bf16 %[acc1], v[212:215], v[220:223], %[acc1]\n" \
"s_add_u32 m0, %[sd], 24576\n" \
"s_mov_b32 %[sf], 1572864\n" \
"buffer_load_dwordx4 %[of0], %[w1r], %[sf] offen lds\n" \
"ds_read_b64_tr_b16 v[200:201], %[va2] offset:12288\n" \
"ds_read_b64_tr_b16 v[202:203], %[va2] offset:13824\n" \
"ds_read_b128 v[204:207], %[vb] offset:832\n" \
"ds_read_b128 v[208:211], %[vb] offset:42304\n" \
"s_waitcnt lgkmcnt(8)\n" \
"v_mfma_f32_32x32x16_bf16 %[acc0], v[176:179], v[180:183], %[acc0]\n" \
"v_mfma_f32_32x32x16_bf16 %[acc1], v[176:179], v[184:187], %[acc1]\n" \
"s_add_u32 m0, %[sd], 25600\n" \
"s_nop 0\n" \
"buffer_load_dwordx4 %[of1], %[w1r], %[sf] offen lds\n" \
"ds_read_b64_tr_b16 v[212:213], %[va2] offset:18432\n" \
"ds_read_b64_tr_b16 v[214:215], %[va2] offset:19968\n" \
"ds_read_b128 v[216:219], %[vb] offset:864\n" \
"ds_read_b128 v[220:223], %[vb] offset:42336\n" \
"s_waitcnt lgkmcnt(8)\n" \
"v_mfma_f32_32x32x16_bf16 %[acc0], v[188:191], v[192:195], %[acc0]\n" \
"v_mfma_f32_32x32x16_bf16 %[acc1], v[188:191], v[196:199], %[acc1]\n" \
"s_add_u32 m0, %[sd], 26624\n" \
"s_nop 0\n" \
"buffer_load_dwordx4 %[of2], %[w1r], %[sf] offen lds\n" \
"s_waitcnt vmcnt(3) lgkmcnt(0)\n" \
"s_barrier\n" \
"ds_read_b64_tr_b16 v[176:177], %[va] offset:0\n" \
"ds_read_b64_tr_b16 v[178:179], %[va] offset:1536\n" \
"ds_read_b128 v[180:183], %[vb] offset:896\n" \
"ds_read_b128 v[184:187], %[vb] offset:42368\n" \
"ds_read_b64_tr_b16 v[188:189], %[va] offset:6144\n" \
"ds_read_b64_tr_b16 v[190:191], %[va] offset:7680\n" \
"ds_read_b128 v[192:195], %[vb] offset:928\n" \
"ds_read_b128 v[196:199], %[vb] offset:42400\n" \
"v_mfma_f32_32x32x16_bf16 %[acc0], v[200:203], v[204:207], %[acc0]\n" \
"v_mfma_f32_32x32x16_bf16 %[acc1], v[200:203], v[208:211], %[acc1]\n" \
"v_mfma_f32_32x32x16_bf16 %[acc0], v[212:215], v[216:219], %[acc0]\n" \
"v_mfma_f32_32x32x16_bf16 %[acc1], v[212:215], v[220:223], %[acc1]\n" \
"s_add_u32 m0, %[sd], 49152\n" \
"s_mov_b32 %[sf], 1769472\n" \
"buffer_load_dwordx4 %[of0], %[w1r], %[sf] offen lds\n" \
"ds_read_b64_tr_b16 v[200:201], %[va] offset:12288\n" \
"ds_read_b64_tr_b16 v[202:203], %[va] offset:13824\n" \
"ds_read_b128 v[204:207], %[vb] offset:960\n" \
"ds_read_b128 v[208:211], %[vb] offset:42432\n" \
"s_waitcnt lgkmcnt(8)\n" \
"v_mfma_f32_32x32x16_bf16 %[acc0], v[176:179], v[180:183], %[acc0]\n" \
"v_mfma_f32_32x32x16_bf16 %[acc1], v[176:179], v[184:187], %[acc1]\n" \
"s_add_u32 m0, %[sd], 50176\n" \
"s_nop 0\n" \
"buffer_load_dwordx4 %[of1], %[w1r], %[sf] offen lds\n" \
"ds_read_b64_tr_b16 v[212:213], %[va] offset:18432\n" \
"ds_read_b64_tr_b16 v[214:215], %[va] offset:19968\n" \
"ds_read_b128 v[216:219], %[vb] offset:992\n" \
"ds_read_b128 v[220:223], %[vb] offset:42464\n" \
"s_waitcnt lgkmcnt(8)\n" \
"v_mfma_f32_32x32x16_bf16 %[acc0], v[188:191], v[192:195], %[acc0]\n" \
"v_mfma_f32_32x32x16_bf16 %[acc1], v[188:191], v[196:199], %[acc1]\n" \
"s_add_u32 m0, %[sd], 51200\n" \
"s_nop 0\n" \
"buffer_load_dwordx4 %[of2], %[w1r], %[sf] offen lds\n" \
"s_mov_b32 %[spin], 0x8000\n" \
"102:\n" \
"v_cmp_eq_u32 vcc, s46, v148\n" \
"v_cmp_eq_u32 %[tmp], s47, v149\n" \
"s_nop 1\n" \
"s_and_b64 vcc, vcc, %[tmp]\n" \
"s_cmp_eq_u64 vcc, -1\n" \
"s_cbranch_scc1 103f\n" \
"s_sub_u32 %[spin], %[spin], 1\n" \
"s_cmp_eq_u32 %[spin], 0\n" \
"s_cbranch_scc1 103f\n" \
"s_sleep 2\n" \
"buffer_load_dwordx2 v[148:149], %[fld], s[56:59], 0 offen sc1\n" \
"s_waitcnt vmcnt(0)\n" \
"s_branch 102b\n" \
"103:\n" \
"s_waitcnt vmcnt(3) lgkmcnt(0)\n" \
"s_barrier\n" \
"ds_read_b64_tr_b16 v[176:177], %[va] offset:24576\n" \
"ds_read_b64_tr_b16 v[178:179], %[va] offset:26112\n" \
"ds_read_b128 v[180:183], %[vb] offset:1024\n" \
"ds_read_b128 v[184:187], %[vb] offset:42496\n" \
"ds_read_b64_tr_b16 v[188:189], %[va] offset:30720\n" \
"ds_read_b64_tr_b16 v[190:191], %[va] offset:32256\n" \
"ds_read_b128 v[192:195], %[vb] offset:1056\n" \
"ds_read_b128 v[196:199], %[vb] offset:42528\n" \
"buffer_load_dwordx4 v[128:131], %[a16], s[56:59], s40 offen offset:0 sc1\n" \
"buffer_load_dwordx2 v[132:133], %[a16], s[56:59], s40 offen offset:3072 sc1\n" \
"buffer_load_dwordx4 v[140:143], %[a16], s[56:59], s40 offen offset:1024 sc1\n" \
"buffer_load_dwordx4 v[144:147], %[a16], s[56:59], s40 offen offset:2048 sc1\n" \
"s_add_u32 s40, s40, 4096\n" \
"buffer_load_dwordx4 v[134:137], %[a16], s[56:59], s40 offen offset:0 sc1\n" \
"buffer_load_dwordx2 v[138:139], %[a16], s[56:59], s40 offen offset:3072 sc1\n" \
"s_cmp_lg_u32 %[wid], 3\n" \
"s_cbranch_scc1 105f\n" \
"s_mov_b32 m0, %[smp]\n" \
"s_mov_b32 %[sf], 0x10000\n" \
"buffer_load_dwordx4 %[a16], s[56:59], %[sf] offen sc1 lds\n" \
"s_add_u32 m0, m0, 1024\n" \
"s_mov_b32 %[sf], 0x10c00\n" \
"buffer_load_dwordx4 %[a16], s[56:59], %[sf] offen sc1 lds\n" \
"105:\n" \
"s_cmp_lg_u32 %[wid], 2\n" \
"s_cbranch_scc1 106f\n" \
"s_mov_b32 m0, %[dum]\n" \
"s_lshl_b32 %[sf], %[shr], 13\n" \
"s_add_u32 %[sf], %[sf], 0x10000\n" \
"v_lshlrev_b32 v150, 3, %[a16]\n" \
"buffer_load_dword v150, s[56:59], %[sf] offen sc1 lds\n" \
"106:\n" \
"v_mfma_f32_32x32x16_bf16 %[acc0], v[200:203], v[204:207], %[acc0]\n" \
"v_mfma_f32_32x32x16_bf16 %[acc1], v[200:203], v[208:211], %[acc1]\n" \
"v_mfma_f32_32x32x16_bf16 %[acc0], v[212:215], v[216:219], %[acc0]\n" \
"v_mfma_f32_32x32x16_bf16 %[acc1], v[212:215], v[220:223], %[acc1]\n" \
"ds_read_b64_tr_b16 v[200:201], %[va] offset:36864\n" \
"ds_read_b64_tr_b16 v[202:203], %[va] offset:38400\n" \
"ds_read_b128 v[204:207], %[vb] offset:1088\n" \
"ds_read_b128 v[208:211], %[vb] offset:42560\n" \
"s_waitcnt lgkmcnt(8)\n" \
"v_mfma_f32_32x32x16_bf16 %[acc0], v[176:179], v[180:183], %[acc0]\n" \
"v_mfma_f32_32x32x16_bf16 %[acc1], v[176:179], v[184:187], %[acc1]\n" \
"ds_read_b64_tr_b16 v[212:213], %[va] offset:43008\n" \
"ds_read_b64_tr_b16 v[214:215], %[va] offset:44544\n" \
"ds_read_b128 v[216:219], %[vb] offset:1120\n" \
"ds_read_b128 v[220:223], %[vb] offset:42592\n" \
"s_waitcnt lgkmcnt(8)\n" \
"v_mfma_f32_32x32x16_bf16 %[acc0], v[188:191], v[192:195], %[acc0]\n" \
"v_mfma_f32_32x32x16_bf16 %[acc1], v[188:191], v[196:199], %[acc1]\n" \
"s_waitcnt vmcnt(6) lgkmcnt(0)\n" \
"s_barrier\n" \
"ds_read_b64_tr_b16 v[176:177], %[va2] offset:0\n" \
"ds_read_b64_tr_b16 v[178:179], %[va2] offset:1536\n" \
"ds_read_b128 v[180:183], %[vb] offset:1152\n" \
"ds_read_b128 v[184:187], %[vb] offset:42624\n" \
"ds_read_b64_tr_b16 v[188:189], %[va2] offset:6144\n" \
"ds_read_b64_tr_b16 v[190:191], %[va2] offset:7680\n" \
"ds_read_b128 v[192:195], %[vb] offset:1184\n" \
"ds_read_b128 v[196:199], %[vb] offset:42656\n" \
"v_mfma_f32_32x32x16_bf16 %[acc0], v[200:203], v[204:207], %[acc0]\n" \
"v_mfma_f32_32x32x16_bf16 %[acc1], v[200:203], v[208:211], %[acc1]\n" \
"v_mfma_f32_32x32x16_bf16 %[acc0], v[212:215], v[216:219], %[acc0]\n" \
"v_mfma_f32_32x32x16_bf16 %[acc1], v[212:215], v[220:223], %[acc1]\n" \
"ds_read_b64_tr_b16 v[200:201], %[va2] offset:12288\n" \
"ds_read_b64_tr_b16 v[202:203], %[va2] offset:13824\n" \
"ds_read_b128 v[204:207], %[vb] offset:1216\n" \
"ds_read_b128 v[208:211], %[vb] offset:42688\n" \
"s_waitcnt lgkmcnt(8)\n" \
"v_mfma_f32_32x32x16_bf16 %[acc0], v[176:179], v[180:183], %[acc0]\n" \
"v_mfma_f32_32x32x16_bf16 %[acc1], v[176:179], v[184:187], %[acc1]\n" \
"ds_read_b64_tr_b16 v[212:213], %[va2] offset:18432\n" \
"ds_read_b64_tr_b16 v[214:215], %[va2] offset:19968\n" \
"ds_read_b128 v[216:219], %[vb] offset:1248\n" \
"ds_read_b128 v[220:223], %[vb] offset:42720\n" \
"s_waitcnt lgkmcnt(8)\n" \
"v_mfma_f32_32x32x16_bf16 %[acc0], v[188:191], v[192:195], %[acc0]\n" \
"v_mfma_f32_32x32x16_bf16 %[acc1], v[188:191], v[196:199], %[acc1]\n" \
"s_waitcnt lgkmcnt(0)\n" \
"v_mfma_f32_32x32x16_bf16 %[acc0], v[200:203], v[204:207], %[acc0]\n" \
"v_mfma_f32_32x32x16_bf16 %[acc1], v[200:203], v[208:211], %[acc1]\n" \
"v_mfma_f32_32x32x16_bf16 %[acc0], v[212:215], v[216:219], %[acc0]\n" \
"v_mfma_f32_32x32x16_bf16 %[acc1], v[212:215], v[220:223], %[acc1]\n" \
"s_branch 31f\n" \
"30:\n" \
"s_waitcnt vmcnt(3) lgkmcnt(0)\n" \
"s_barrier\n" \
"ds_read_b64_tr_b16 v[176:177], %[va2] offset:0\n" \
"ds_read_b64_tr_b16 v[178:179], %[va2] offset:1536\n" \
"ds_read_b128 v[180:183], %[vb] offset:0\n" \
"ds_read_b64_tr_b16 v[184:185], %[va2] offset:6144\n" \
"ds_read_b64_tr_b16 v[186:187], %[va2] offset:7680\n" \
"ds_read_b128 v[188:191], %[vb] offset:32\n" \
"s_add_u32 m0, %[sd], 24576\n" \
"s_mov_b32 %[sf], 393216\n" \
"buffer_load_dwordx4 %[of0], %[w1r], %[sf] offen lds\n" \
"ds_read_b64_tr_b16 v[192:193], %[va2] offset:12288\n" \
"ds_read_b64_tr_b16 v[194:195], %[va2] offset:13824\n" \
"ds_read_b128 v[196:199], %[vb] offset:64\n" \
"s_waitcnt lgkmcnt(6)\n" \
"v_mfma_f32_32x32x16_bf16 %[acc0], v[176:179], v[180:183], %[bias]\n" \
"s_add_u32 m0, %[sd], 25600\n" \
"s_nop 0\n" \
"buffer_load_dwordx4 %[of1], %[w1r], %[sf] offen lds\n" \
"ds_read_b64_tr_b16 v[200:201], %[va2] offset:18432\n" \
"ds_read_b64_tr_b16 v[202:203], %[va2] offset:19968\n" \
"ds_read_b128 v[204:207], %[vb] offset:96\n" \
"s_waitcnt lgkmcnt(6)\n" \
"v_mfma_f32_32x32x16_bf16 %[acc0], v[184:187], v[188:191], %[acc0]\n" \
"s_add_u32 m0, %[sd], 26624\n" \
"s_nop 0\n" \
"buffer_load_dwordx4 %[of2], %[w1r], %[sf] offen lds\n" \
"s_waitcnt vmcnt(3) lgkmcnt(0)\n" \
"s_barrier\n" \
"ds_read_b64_tr_b16 v[176:177], %[va] offset:0\n" \
"ds_read_b64_tr_b16 v[178:179], %[va] offset:1536\n" \
"ds_read_b128 v[180:183], %[vb] offset:128\n" \
"ds_read_b64_tr_b16 v[184:185], %[va] offset:6144\n" \
"ds_read_b64_tr_b16 v[186:187], %[va] offset:7680\n" \
"ds_read_b128 v[188:191], %[vb] offset:160\n" \
"v_mfma_f32_32x32x16_bf16 %[acc0], v[192:195], v[196:199], %[acc0]\n" \
"v_mfma_f32_32x32x16_bf16 %[acc0], v[200:203], v[204:207], %[acc0]\n" \
"s_add_u32 m0, %[sd], 49152\n" \
"s_mov_b32 %[sf], 589824\n" \
"buffer_load_dwordx4 %[of0], %[w1r], %[sf] offen lds\n" \
"ds_read_b64_tr_b16 v[192:193], %[va] offset:12288\n" \
"ds_read_b64_tr_b16 v[194:195], %[va] offset:13824\n" \
"ds_read_b128 v[196:199], %[vb] offset:192\n" \
"s_waitcnt lgkmcnt(6)\n" \
"v_mfma_f32_32x32x16_bf16 %[acc0], v[176:179], v[180:183], %[acc0]\n" \
"s_add_u32 m0, %[sd], 50176\n" \
"s_nop 0\n" \
"buffer_load_dwordx4 %[of1], %[w1r], %[sf] offen lds\n" \
"ds_read_b64_tr_b16 v[200:201], %[va] offset:18432\n" \
"ds_read_b64_tr_b16 v[202:203], %[va] offset:19968\n" \
"ds_read_b128 v[204:207], %[vb] offset:224\n" \
"s_waitcnt lgkmcnt(6)\n" \
"v_mfma_f32_32x32x16_bf16 %[acc0], v[184:187], v[188:191], %[acc0]\n" \
"s_add_u32 m0, %[sd], 51200\n" \
"s_nop 0\n" \
"buffer_load_dwordx4 %[of2], %[w1r], %[sf] offen lds\n" \
"s_waitcnt vmcnt(3) lgkmcnt(0)\n" \
"s_barrier\n" \
"ds_read_b64_tr_b16 v[176:177], %[va] offset:24576\n" \
"ds_read_b64_tr_b16 v[178:179], %[va] offset:26112\n" \
"ds_read_b128 v[180:183], %[vb] offset:256\n" \
"ds_read_b64_tr_b16 v[184:185], %[va] offset:30720\n" \
"ds_read_b64_tr_b16 v[186:187], %[va] offset:32256\n" \
"ds_read_b128 v[188:191], %[vb] offset:288\n" \
"v_mfma_f32_32x32x16_bf16 %[acc0], v[192:195], v[196:199], %[acc0]\n" \
"v_mfma_f32_32x32x16_bf16 %[acc0], v[200:203], v[204:207], %[acc0]\n" \
"s_add_u32 m0, %[sd], 0\n" \
"s_mov_b32 %[sf], 786432\n" \
"buffer_load_dwordx4 %[of0], %[w1r], %[sf] offen lds\n" \
"ds_read_b64_tr_b16 v[192:193], %[va] offset:36864\n" \
"ds_read_b64_tr_b16 v[194:195], %[va] offset:38400\n" \
"ds_read_b128 v[196:199], %[vb] offset:320\n" \
"s_waitcnt lgkmcnt(6)\n" \
"v_mfma_f32_32x32x16_bf16 %[acc0], v[176:179], v[180:183], %[acc0]\n" \
"s_add_u32 m0, %[sd], 1024\n" \
"s_nop 0\n" \
"buffer_load_dwordx4 %[of1], %[w1r], %[sf] offen lds\n" \
"ds_read_b64_tr_b16 v[200:201], %[va] offset:43008\n" \
"ds_read_b64_tr_b16 v[202:203], %[va] offset:44544\n" \
"ds_read_b128 v[204:207], %[vb] offset:352\n" \
"s_waitcnt lgkmcnt(6)\n" \
"v_mfma_f32_32x32x16_bf16 %[acc0], v[184:187], v[188:191], %[acc0]\n" \
"s_add_u32 m0, %[sd], 2048\n" \
"s_nop 0\n" \
"buffer_load_dwordx4 %[of2], %[w1r], %[sf] offen lds\n" \
"s_waitcnt vmcnt(3) lgkmcnt(0)\n" \
"s_barrier\n" \
"ds_read_b64_tr_b16 v[176:177], %[va2] offset:0\n" \
"ds_read_b64_tr_b16 v[178:179], %[va2] offset:1536\n" \
"ds_read_b128 v[180:183], %[vb] offset:384\n" \
"ds_read_b64_tr_b16 v[184:185], %[va2] offset:6144\n" \
"ds_read_b64_tr_b16 v[186:187], %[va2] offset:7680\n" \
"ds_read_b128 v[188:191], %[vb] offset:416\n" \
"v_mfma_f32_32x32x16_bf16 %[acc0], v[192:195], v[196:199], %[acc0]\n" \
"v_mfma_f32_32x32x16_bf16 %[acc0], v[200:203], v[204:207], %[acc0]\n" \
"s_add_u32 m0, %[sd], 24576\n" \
"s_mov_b32 %[sf], 983040\n" \
"buffer_load_dwordx4 %[of0], %[w1r], %[sf] offen lds\n" \
"ds_read_b64_tr_b16 v[192:193], %[va2] offset:12288\n" \
"ds_read_b64_tr_b16 v[194:195], %[va2] offset:13824\n" \
"ds_read_b128 v[196:199], %[vb] offset:448\n" \
"s_waitcnt lgkmcnt(6)\n" \
"v_mfma_f32_32x32x16_bf16 %[acc0], v[176:179], v[180:183], %[acc0]\n" \
"s_add_u32 m0, %[sd], 25600\n" \
"s_nop 0\n" \
"buffer_load_dwordx4 %[of1], %[w1r], %[sf] offen lds\n" \
"ds_read_b64_tr_b16 v[200:201], %[va2] offset:18432\n" \
"ds_read_b64_tr_b16 v[202:203], %[va2] offset:19968\n" \
"ds_read_b128 v[204:207], %[vb] offset:480\n" \
"s_waitcnt lgkmcnt(6)\n" \
"v_mfma_f32_32x32x16_bf16 %[acc0], v[184:187], v[188:191], %[acc0]\n" \
"s_add_u32 m0, %[sd], 26624\n" \
"s_nop 0\n" \
"buffer_load_dwordx4 %[of2], %[w1r], %[sf] offen lds\n" \
"s_waitcnt vmcnt(3) lgkmcnt(0)\n" \
"s_barrier\n" \
"ds_read_b64_tr_b16 v[176:177], %[va] offset:0\n" \
"ds_read_b64_tr_b16 v[178:179], %[va] offset:1536\n" \
"ds_read_b128 v[180:183], %[vb] offset:512\n" \
"ds_read_b64_tr_b16 v[184:185], %[va] offset:6144\n" \
"ds_read_b64_tr_b16 v[186:187], %[va] offset:7680\n" \
"ds_read_b128 v[188:191], %[vb] offset:544\n" \
"v_mfma_f32_32x32x16_bf16 %[acc0], v[192:195], v[196:199], %[acc0]\n" \
"v_mfma_f32_32x32x16_bf16 %[acc0], v[200:203], v[204:207], %[acc0]\n" \
"s_add_u32 m0, %[sd], 49152\n" \
"s_mov_b32 %[sf], 1179648\n" \
"buffer_load_dwordx4 %[of0], %[w1r], %[sf] offen lds\n" \
"ds_read_b64_tr_b16 v[192:193], %[va] offset:12288\n" \
"ds_read_b64_tr_b16 v[194:195], %[va] offset:13824\n" \
"ds_read_b128 v[196:199], %[vb] offset:576\n" \
"s_waitcnt lgkmcnt(6)\n" \
"v_mfma_f32_32x32x16_bf16 %[acc0], v[176:179], v[180:183], %[acc0]\n" \
"s_add_u32 m0, %[sd], 50176\n" \
"s_nop 0\n" \
"buffer_load_dwordx4 %[of1], %[w1r], %[sf] offen lds\n" \
"ds_read_b64_tr_b16 v[200:201], %[va] offset:18432\n" \
"ds_read_b64_tr_b16 v[202:203], %[va] offset:19968\n" \
"ds_read_b128 v[204:207], %[vb] offset:608\n" \
"s_waitcnt lgkmcnt(6)\n" \
"v_mfma_f32_32x32x16_bf16 %[acc0], v[184:187], v[188:191], %[acc0]\n" \
"s_add_u32 m0, %[sd], 51200\n" \
"s_nop 0\n" \
"buffer_load_dwordx4 %[of2], %[w1r], %[sf] offen lds\n" \
"s_waitcnt vmcnt(3) lgkmcnt(0)\n" \
"s_barrier\n" \
"ds_read_b64_tr_b16 v[176:177], %[va] offset:24576\n" \
"ds_read_b64_tr_b16 v[178:179], %[va] offset:26112\n" \
"ds_read_b128 v[180:183], %[vb] offset:640\n" \
"ds_read_b64_tr_b16 v[184:185], %[va] offset:30720\n" \
"ds_read_b64_tr_b16 v[186:187], %[va] offset:32256\n" \
"ds_read_b128 v[188:191], %[vb] offset:672\n" \
"buffer_load_dwordx2 v[148:149], %[fld], s[56:59], 0 offen sc1\n" \
"v_mfma_f32_32x32x16_bf16 %[acc0], v[192:195], v[196:199], %[acc0]\n" \
"v_mfma_f32_32x32x16_bf16 %[acc0], v[200:203], v[204:207], %[acc0]\n" \
"s_add_u32 m0, %[sd], 0\n" \
"s_mov_b32 %[sf], 1376256\n" \
"buffer_load_dwordx4 %[of0], %[w1r], %[sf] offen lds\n" \
"ds_read_b64_tr_b16 v[192:193], %[va] offset:36864\n" \
"ds_read_b64_tr_b16 v[194:195], %[va] offset:38400\n" \
"ds_read_b128 v[196:199], %[vb] offset:704\n" \
"s_waitcnt lgkmcnt(6)\n" \
"v_mfma_f32_32x32x16_bf16 %[acc0], v[176:179], v[180:183], %[acc0]\n" \
"s_add_u32 m0, %[sd], 1024\n" \
"s_nop 0\n" \
"buffer_load_dwordx4 %[of1], %[w1r], %[sf] offen lds\n" \
"ds_read_b64_tr_b16 v[200:201], %[va] offset:43008\n" \
"ds_read_b64_tr_b16 v[202:203], %[va] offset:44544\n" \
"ds_read_b128 v[204:207], %[vb] offset:736\n" \
"s_waitcnt lgkmcnt(6)\n" \
"v_mfma_f32_32x32x16_bf16 %[acc0], v[184:187], v[188:191], %[acc0]\n" \
"s_add_u32 m0, %[sd], 2048\n" \
"s_nop 0\n" \
"buffer_load_dwordx4 %[of2], %[w1r], %[sf] offen lds\n" \
"s_waitcnt vmcnt(4) lgkmcnt(0)\n" \
"s_barrier\n" \
"ds_read_b64_tr_b16 v[176:177], %[va2] offset:0\n" \
"ds_read_b64_tr_b16 v[178:179], %[va2] offset:1536\n" \
"ds_read_b128 v[180:183], %[vb] offset:768\n" \
"ds_read_b64_tr_b16 v[184:185], %[va2] offset:6144\n" \
"ds_read_b64_tr_b16 v[186:187], %[va2] offset:7680\n" \
"ds_read_b128 v[188:191], %[vb] offset:800\n" \
"v_mfma_f32_32x32x16_bf16 %[acc0], v[192:195], v[196:199], %[acc0]\n" \
"v_mfma_f32_32x32x16_bf16 %[acc0], v[200:203], v[204:207], %[acc0]\n" \
"s_add_u32 m0, %[sd], 24576\n" \
"s_mov_b32 %[sf], 1572864\n" \
"buffer_load_dwordx4 %[of0], %[w1r], %[sf] offen lds\n" \
"ds_read_b64_tr_b16 v[192:193], %[va2] offset:12288\n" \
"ds_read_b64_tr_b16 v[194:195], %[va2] offset:13824\n" \
"ds_read_b128 v[196:199], %[vb] offset:832\n" \
"s_waitcnt lgkmcnt(6)\n" \
"v_mfma_f32_32x32x16_bf16 %[acc0], v[176:179], v[180:183], %[acc0]\n" \
"s_add_u32 m0, %[sd], 25600\n" \
"s_nop 0\n" \
"buffer_load_dwordx4 %[of1], %[w1r], %[sf] offen lds\n" \
"ds_read_b64_tr_b16 v[200:201], %[va2] offset:18432\n" \
"ds_read_b64_tr_b16 v[202:203], %[va2] offset:19968\n" \
"ds_read_b128 v[204:207], %[vb] offset:864\n" \
"s_waitcnt lgkmcnt(6)\n" \
"v_mfma_f32_32x32x16_bf16 %[acc0], v[184:187], v[188:191], %[acc0]\n" \
"s_add_u32 m0, %[sd], 26624\n" \
"s_nop 0\n" \
"buffer_load_dwordx4 %[of2], %[w1r], %[sf] offen lds\n" \
"s_waitcnt vmcnt(3) lgkmcnt(0)\n" \
"s_barrier\n" \
"ds_read_b64_tr_b16 v[176:177], %[va] offset:0\n" \
"ds_read_b64_tr_b16 v[178:179], %[va] offset:1536\n" \
"ds_read_b128 v[180:183], %[vb] offset:896\n" \
"ds_read_b64_tr_b16 v[184:185], %[va] offset:6144\n" \
"ds_read_b64_tr_b16 v[186:187], %[va] offset:7680\n" \
"ds_read_b128 v[188:191], %[vb] offset:928\n" \
"v_mfma_f32_32x32x16_bf16 %[acc0], v[192:195], v[196:199], %[acc0]\n" \
"v_mfma_f32_32x32x16_bf16 %[acc0], v[200:203], v[204:207], %[acc0]\n" \
"s_add_u32 m0, %[sd], 49152\n" \
"s_mov_b32 %[sf], 1769472\n" \
"buffer_load_dwordx4 %[of0], %[w1r], %[sf] offen lds\n" \
"ds_read_b64_tr_b16 v[192:193], %[va] offset:12288\n" \
"ds_read_b64_tr_b16 v[194:195], %[va] offset:13824\n" \
"ds_read_b128 v[196:199], %[vb] offset:960\n" \
"s_waitcnt lgkmcnt(6)\n" \
"v_mfma_f32_32x32x16_bf16 %[acc0], v[176:179], v[180:183], %[acc0]\n" \
"s_add_u32 m0, %[sd], 50176\n" \
"s_nop 0\n" \
"buffer_load_dwordx4 %[of1], %[w1r], %[sf] offen lds\n" \
"ds_read_b64_tr_b16 v[200:201], %[va] offset:18432\n" \
"ds_read_b64_tr_b16 v[202:203], %[va] offset:19968\n" \
"ds_read_b128 v[204:207], %[vb] offset:992\n" \
"s_waitcnt lgkmcnt(6)\n" \
"v_mfma_f32_32x32x16_bf16 %[acc0], v[184:187], v[188:191], %[acc0]\n" \
"s_add_u32 m0, %[sd], 51200\n" \
"s_nop 0\n" \
"buffer_load_dwordx4 %[of2], %[w1r], %[sf] offen lds\n" \
"s_mov_b32 %[spin], 0x8000\n" \
"202:\n" \
"v_cmp_eq_u32 vcc, s46, v148\n" \
"v_cmp_eq_u32 %[tmp], s47, v149\n" \
"s_nop 1\n" \
"s_and_b64 vcc, vcc, %[tmp]\n" \
"s_cmp_eq_u64 vcc, -1\n" \
"s_cbranch_scc1 203f\n" \
"s_sub_u32 %[spin], %[spin], 1\n" \
"s_cmp_eq_u32 %[spin], 0\n" \
"s_cbranch_scc1 203f\n" \
"s_sleep 2\n" \
"buffer_load_dwordx2 v[148:149], %[fld], s[56:59], 0 offen sc1\n" \
"s_waitcnt vmcnt(0)\n" \
"s_branch 202b\n" \
"203:\n" \
"s_waitcnt vmcnt(3) lgkmcnt(0)\n" \
"s_barrier\n" \
"ds_read_b64_tr_b16 v[176:177], %[va] offset:24576\n" \
"ds_read_b64_tr_b16 v[178:179], %[va] offset:26112\n" \
"ds_read_b128 v[180:183], %[vb] offset:1024\n" \
"ds_read_b64_tr_b16 v[184:185], %[va] offset:30720\n" \
"ds_read_b64_tr_b16 v[186:187], %[va] offset:32256\n" \
"ds_read_b128 v[188:191], %[vb] offset:1056\n" \
"buffer_load_dwordx4 v[128:131], %[a16], s[56:59], s40 offen offset:0 sc1\n" \
"buffer_load_dwordx2 v[132:133], %[a16], s[56:59], s40 offen offset:3072 sc1\n" \
"buffer_load_dwordx4 v[140:143], %[a16], s[56:59], s40 offen offset:1024 sc1\n" \
"buffer_load_dwordx4 v[144:147], %[a16], s[56:59], s40 offen offset:2048 sc1\n" \
"s_add_u32 s40, s40, 4096\n" \
"buffer_load_dwordx4 v[134:137], %[a16], s[56:59], s40 offen offset:0 sc1\n" \
"buffer_load_dwordx2 v[138:139], %[a16], s[56:59], s40 offen offset:3072 sc1\n" \
"v_mfma_f32_32x32x16_bf16 %[acc0], v[192:195], v[196:199], %[acc0]\n" \
"v_mfma_f32_32x32x16_bf16 %[acc0], v[200:203], v[204:207], %[acc0]\n" \
"ds_read_b64_tr_b16 v[192:193], %[va] offset:36864\n" \
"ds_read_b64_tr_b16 v[194:195], %[va] offset:38400\n" \
"ds_read_b128 v[196:199], %[vb] offset:1088\n" \
"s_waitcnt lgkmcnt(6)\n" \
"v_mfma_f32_32x32x16_bf16 %[acc0], v[176:179], v[180:183], %[acc0]\n" \
"ds_read_b64_tr_b16 v[200:201], %[va] offset:43008\n" \
"ds_read_b64_tr_b16 v[202:203], %[va] offset:44544\n" \
"ds_read_b128 v[204:207], %[vb] offset:1120\n" \
"s_waitcnt lgkmcnt(6)\n" \
"v_mfma_f32_32x32x16_bf16 %[acc0], v[184:187], v[188:191], %[acc0]\n" \
"s_waitcnt vmcnt(6) lgkmcnt(0)\n" \
"s_barrier\n" \
"ds_read_b64_tr_b16 v[176:177], %[va2] offset:0\n" \
"ds_read_b64_tr_b16 v[178:179], %[va2] offset:1536\n" \
"ds_read_b128 v[180:183], %[vb] offset:1152\n" \
"ds_read_b64_tr_b16 v[184:185], %[va2] offset:6144\n" \
"ds_read_b64_tr_b16 v[186:187], %[va2] offset:7680\n" \
"ds_read_b128 v[188:191], %[vb] offset:1184\n" \
"s_cmp_eq_u32 %[dop], 0\n" \
"s_cbranch_scc1 207f\n" \
"s_add_u32 m0, %[rpd], 0\n" \
"s_nop 0\n" \
"buffer_load_dwordx4 %[rp0], %[rpr], %[rps] offen lds\n" \
"s_add_u32 m0, %[rpd], 1024\n" \
"s_nop 0\n" \
"buffer_load_dwordx4 %[rp1], %[rpr], %[rps] offen lds\n" \
"s_add_u32 m0, %[rpd], 2048\n" \
"s_nop 0\n" \
"buffer_load_dwordx4 %[rp2], %[rpr], %[rps] offen lds\n" \
"s_add_u32 m0, %[rpd], 3072\n" \
"s_nop 0\n" \
"buffer_load_dwordx4 %[rp3], %[rpr], %[rps] offen lds\n" \
"s_add_u32 m0, %[rpd], 4096\n" \
"s_nop 0\n" \
"buffer_load_dwordx4 %[rp4], %[rpr], %[rps] offen lds\n" \
"207:\n" \
"v_mfma_f32_32x32x16_bf16 %[acc0], v[192:195], v[196:199], %[acc0]\n" \
"v_mfma_f32_32x32x16_bf16 %[acc0], v[200:203], v[204:207], %[acc0]\n" \
"ds_read_b64_tr_b16 v[192:193], %[va2] offset:12288\n" \
"ds_read_b64_tr_b16 v[194:195], %[va2] offset:13824\n" \
"ds_read_b128 v[196:199], %[vb] offset:1216\n" \
"s_waitcnt lgkmcnt(6)\n" \
"v_mfma_f32_32x32x16_bf16 %[acc0], v[176:179], v[180:183], %[acc0]\n" \
"ds_read_b64_tr_b16 v[200:201], %[va2] offset:18432\n" \
"ds_read_b64_tr_b16 v[202:203], %[va2] offset:19968\n" \
"ds_read_b128 v[204:207], %[vb] offset:1248\n" \
"s_waitcnt lgkmcnt(6)\n" \
"v_mfma_f32_32x32x16_bf16 %[acc0], v[184:187], v[188:191], %[acc0]\n" \
"s_waitcnt lgkmcnt(0)\n" \
"v_mfma_f32_32x32x16_bf16 %[acc0], v[192:195], v[196:199], %[acc0]\n" \
"v_mfma_f32_32x32x16_bf16 %[acc0], v[200:203], v[204:207], %[acc0]\n" \
"31:\n" \
"s_mov_b32 m0, %[keep]\n" \
"s_nop 7\n" \
"s_nop 7\n"

#define C_ASM \
"s_mov_b32 %[keep], m0\n" \
"s_mov_b32 vcc_lo, %[lo4]\n" \
"s_mov_b32 vcc_hi, %[lo4]\n" \
"ds_read_b128 v[40:43], %[apl] offset:0\n" \
"ds_read_b128 v[44:47], %[apl] offset:1024\n" \
"ds_read_b128 v[48:51], %[apl] offset:2048\n" \
"ds_read_b128 v[52:55], %[apl] offset:3072\n" \
"ds_read_b128 v[56:59], %[apl] offset:4096\n" \
"ds_read_b128 v[60:63], %[apl] offset:20480\n" \
"ds_read_b128 v[64:67], %[apl] offset:21504\n" \
"ds_read_b128 v[68:71], %[apl] offset:22528\n" \
"ds_read_b128 v[72:75], %[apl] offset:23552\n" \
"ds_read_b128 v[76:79], %[apl] offset:24576\n" \
"s_waitcnt lgkmcnt(5)\n" \
"v_mfma_f32_16x16x32_bf16 v[0:3], v[40:43], v[168:171], 0\n" \
"v_mfma_f32_16x16x32_bf16 v[4:7], v[40:43], v[172:175], 0\n" \
"s_add_u32 m0, %[sd], 81920\n" \
"s_nop 0\n" \
"buffer_load_dwordx4 %[a16], s[56:59], %[so] offen sc1 lds\n" \
"v_mfma_f32_16x16x32_bf16 v[8:11], v[44:47], v[168:171], 0\n" \
"v_mfma_f32_16x16x32_bf16 v[12:15], v[44:47], v[172:175], 0\n" \
"s_add_u32 m0, %[sd], 82944\n" \
"s_add_u32 %[so4], %[so], 1024\n" \
"buffer_load_dwordx4 %[a16], s[56:59], %[so4] offen sc1 lds\n" \
"v_mfma_f32_16x16x32_bf16 v[16:19], v[48:51], v[168:171], 0\n" \
"v_mfma_f32_16x16x32_bf16 v[20:23], v[48:51], v[172:175], 0\n" \
"s_add_u32 m0, %[sd], 83968\n" \
"s_add_u32 %[so4], %[so], 2048\n" \
"buffer_load_dwordx4 %[a16], s[56:59], %[so4] offen sc1 lds\n" \
"v_mfma_f32_16x16x32_bf16 v[24:27], v[52:55], v[168:171], 0\n" \
"v_mfma_f32_16x16x32_bf16 v[28:31], v[52:55], v[172:175], 0\n" \
"s_add_u32 m0, %[sd], 84992\n" \
"s_add_u32 %[so4], %[so], 3072\n" \
"buffer_load_dwordx4 %[a16], s[56:59], %[so4] offen sc1 lds\n" \
"v_mfma_f32_16x16x32_bf16 v[32:35], v[56:59], v[168:171], 0\n" \
"v_mfma_f32_16x16x32_bf16 v[36:39], v[56:59], v[172:175], 0\n" \
"s_add_u32 m0, %[sd], 86016\n" \
"s_add_u32 %[so4], %[so], 4096\n" \
"buffer_load_dwordx4 %[a16], s[56:59], %[so4] offen sc1 lds\n" \
"s_add_u32 %[so], %[so], 10240\n" \
"s_add_u32 m0, %[sd], 122880\n" \
"s_nop 0\n" \
"buffer_load_dwordx4 %[a16], s[56:59], %[so] offen sc1 lds\n" \
"s_add_u32 m0, %[sd], 123904\n" \
"s_add_u32 %[so4], %[so], 1024\n" \
"buffer_load_dwordx4 %[a16], s[56:59], %[so4] offen sc1 lds\n" \
"s_add_u32 m0, %[sd], 124928\n" \
"s_add_u32 %[so4], %[so], 2048\n" \
"buffer_load_dwordx4 %[a16], s[56:59], %[so4] offen sc1 lds\n" \
"s_add_u32 m0, %[sd], 125952\n" \
"s_add_u32 %[so4], %[so], 3072\n" \
"buffer_load_dwordx4 %[a16], s[56:59], %[so4] offen sc1 lds\n" \
"s_add_u32 m0, %[sd], 126976\n" \
"s_add_u32 %[so4], %[so], 4096\n" \
"buffer_load_dwordx4 %[a16], s[56:59], %[so4] offen sc1 lds\n" \
"s_waitcnt vmcnt(10)\n" \
"s_mov_b64 exec, 0xff\n" \
"buffer_store_dwordx2 %[tagv], %[fst], s[56:59], 0 offen sc1\n" \
"s_mov_b64 exec, -1\n" \
"s_waitcnt lgkmcnt(0)\n" \
"s_barrier\n" \
"ds_read_b128 v[40:43], %[apl] offset:40960\n" \
"ds_read_b128 v[44:47], %[apl] offset:41984\n" \
"ds_read_b128 v[48:51], %[apl] offset:43008\n" \
"ds_read_b128 v[52:55], %[apl] offset:44032\n" \
"ds_read_b128 v[56:59], %[apl] offset:45056\n" \
"v_mfma_f32_16x16x32_bf16 v[0:3], v[60:63], v[176:179], v[0:3]\n" \
"v_mfma_f32_16x16x32_bf16 v[4:7], v[60:63], v[180:183], v[4:7]\n" \
"v_mfma_f32_16x16x32_bf16 v[8:11], v[64:67], v[176:179], v[8:11]\n" \
"v_mfma_f32_16x16x32_bf16 v[12:15], v[64:67], v[180:183], v[12:15]\n" \
"s_add_u32 %[so], %[so], 10240\n" \
"s_add_u32 m0, %[sd], 0\n" \
"s_nop 0\n" \
"buffer_load_dwordx4 %[a16], s[56:59], %[so] offen sc1 lds\n" \
"v_mfma_f32_16x16x32_bf16 v[16:19], v[68:71], v[176:179], v[16:19]\n" \
"v_mfma_f32_16x16x32_bf16 v[20:23], v[68:71], v[180:183], v[20:23]\n" \
"v_mfma_f32_16x16x32_bf16 v[24:27], v[72:75], v[176:179], v[24:27]\n" \
"v_mfma_f32_16x16x32_bf16 v[28:31], v[72:75], v[180:183], v[28:31]\n" \
"s_add_u32 m0, %[sd], 1024\n" \
"s_add_u32 %[so4], %[so], 1024\n" \
"buffer_load_dwordx4 %[a16], s[56:59], %[so4] offen sc1 lds\n" \
"v_mfma_f32_16x16x32_bf16 v[32:35], v[76:79], v[176:179], v[32:35]\n" \
"v_mfma_f32_16x16x32_bf16 v[36:39], v[76:79], v[180:183], v[36:39]\n" \
"ds_read_b128 v[60:63], %[apm] offset:0\n" \
"ds_read_b128 v[64:67], %[apm] offset:1024\n" \
"ds_read_b128 v[68:71], %[apm] offset:2048\n" \
"ds_read_b128 v[72:75], %[apm] offset:3072\n" \
"ds_read_b128 v[76:79], %[apm] offset:4096\n" \
"s_waitcnt lgkmcnt(5)\n" \
"v_mfma_f32_16x16x32_bf16 v[0:3], v[40:43], v[184:187], v[0:3]\n" \
"v_mfma_f32_16x16x32_bf16 v[4:7], v[40:43], v[188:191], v[4:7]\n" \
"s_add_u32 m0, %[sd], 2048\n" \
"s_add_u32 %[so4], %[so], 2048\n" \
"buffer_load_dwordx4 %[a16], s[56:59], %[so4] offen sc1 lds\n" \
"v_mfma_f32_16x16x32_bf16 v[8:11], v[44:47], v[184:187], v[8:11]\n" \
"v_mfma_f32_16x16x32_bf16 v[12:15], v[44:47], v[188:191], v[12:15]\n" \
"v_mfma_f32_16x16x32_bf16 v[16:19], v[48:51], v[184:187], v[16:19]\n" \
"v_mfma_f32_16x16x32_bf16 v[20:23], v[48:51], v[188:191], v[20:23]\n" \
"s_add_u32 m0, %[sd], 3072\n" \
"s_add_u32 %[so4], %[so], 3072\n" \
"buffer_load_dwordx4 %[a16], s[56:59], %[so4] offen sc1 lds\n" \
"v_mfma_f32_16x16x32_bf16 v[24:27], v[52:55], v[184:187], v[24:27]\n" \
"v_mfma_f32_16x16x32_bf16 v[28:31], v[52:55], v[188:191], v[28:31]\n" \
"v_mfma_f32_16x16x32_bf16 v[32:35], v[56:59], v[184:187], v[32:35]\n" \
"v_mfma_f32_16x16x32_bf16 v[36:39], v[56:59], v[188:191], v[36:39]\n" \
"s_add_u32 m0, %[sd], 4096\n" \
"s_add_u32 %[so4], %[so], 4096\n" \
"buffer_load_dwordx4 %[a16], s[56:59], %[so4] offen sc1 lds\n" \
"s_waitcnt vmcnt(11) lgkmcnt(0)\n" \
"s_barrier\n" \
"ds_read_b128 v[40:43], %[apm] offset:20480\n" \
"ds_read_b128 v[44:47], %[apm] offset:21504\n" \
"ds_read_b128 v[48:51], %[apm] offset:22528\n" \
"ds_read_b128 v[52:55], %[apm] offset:23552\n" \
"ds_read_b128 v[56:59], %[apm] offset:24576\n" \
"v_mfma_f32_16x16x32_bf16 v[0:3], v[60:63], v[192:195], v[0:3]\n" \
"v_mfma_f32_16x16x32_bf16 v[4:7], v[60:63], v[196:199], v[4:7]\n" \
"v_mfma_f32_16x16x32_bf16 v[8:11], v[64:67], v[192:195], v[8:11]\n" \
"v_mfma_f32_16x16x32_bf16 v[12:15], v[64:67], v[196:199], v[12:15]\n" \
"s_add_u32 %[so], %[so], 10240\n" \
"s_add_u32 m0, %[sd], 40960\n" \
"s_nop 0\n" \
"buffer_load_dwordx4 %[a16], s[56:59], %[so] offen sc1 lds\n" \
"v_mfma_f32_16x16x32_bf16 v[16:19], v[68:71], v[192:195], v[16:19]\n" \
"v_mfma_f32_16x16x32_bf16 v[20:23], v[68:71], v[196:199], v[20:23]\n" \
"v_mfma_f32_16x16x32_bf16 v[24:27], v[72:75], v[192:195], v[24:27]\n" \
"v_mfma_f32_16x16x32_bf16 v[28:31], v[72:75], v[196:199], v[28:31]\n" \
"s_add_u32 m0, %[sd], 41984\n" \
"s_add_u32 %[so4], %[so], 1024\n" \
"buffer_load_dwordx4 %[a16], s[56:59], %[so4] offen sc1 lds\n" \
"v_mfma_f32_16x16x32_bf16 v[32:35], v[76:79], v[192:195], v[32:35]\n" \
"v_mfma_f32_16x16x32_bf16 v[36:39], v[76:79], v[196:199], v[36:39]\n" \
"ds_read_b128 v[60:63], %[apm] offset:40960\n" \
"ds_read_b128 v[64:67], %[apm] offset:41984\n" \
"ds_read_b128 v[68:71], %[apm] offset:43008\n" \
"ds_read_b128 v[72:75], %[apm] offset:44032\n" \
"ds_read_b128 v[76:79], %[apm] offset:45056\n" \
"s_waitcnt lgkmcnt(5)\n" \
"v_mfma_f32_16x16x32_bf16 v[0:3], v[40:43], v[200:203], v[0:3]\n" \
"v_mfma_f32_16x16x32_bf16 v[4:7], v[40:43], v[204:207], v[4:7]\n" \
"s_add_u32 m0, %[sd], 43008\n" \
"s_add_u32 %[so4], %[so], 2048\n" \
"buffer_load_dwordx4 %[a16], s[56:59], %[so4] offen sc1 lds\n" \
"v_mfma_f32_16x16x32_bf16 v[8:11], v[44:47], v[200:203], v[8:11]\n" \
"v_mfma_f32_16x16x32_bf16 v[12:15], v[44:47], v[204:207], v[12:15]\n" \
"v_mfma_f32_16x16x32_bf16 v[16:19], v[48:51], v[200:203], v[16:19]\n" \
"v_mfma_f32_16x16x32_bf16 v[20:23], v[48:51], v[204:207], v[20:23]\n" \
"s_add_u32 m0, %[sd], 44032\n" \
"s_add_u32 %[so4], %[so], 3072\n" \
"buffer_load_dwordx4 %[a16], s[56:59], %[so4] offen sc1 lds\n" \
"v_mfma_f32_16x16x32_bf16 v[24:27], v[52:55], v[200:203], v[24:27]\n" \
"v_mfma_f32_16x16x32_bf16 v[28:31], v[52:55], v[204:207], v[28:31]\n" \
"v_mfma_f32_16x16x32_bf16 v[32:35], v[56:59], v[200:203], v[32:35]\n" \
"v_mfma_f32_16x16x32_bf16 v[36:39], v[56:59], v[204:207], v[36:39]\n" \
"s_add_u32 m0, %[sd], 45056\n" \
"s_add_u32 %[so4], %[so], 4096\n" \
"buffer_load_dwordx4 %[a16], s[56:59], %[so4] offen sc1 lds\n" \
"s_waitcnt vmcnt(11) lgkmcnt(0)\n" \
"s_barrier\n" \
"ds_read_b128 v[40:43], %[aph] offset:0\n" \
"ds_read_b128 v[44:47], %[aph] offset:1024\n" \
"ds_read_b128 v[48:51], %[aph] offset:2048\n" \
"ds_read_b128 v[52:55], %[aph] offset:3072\n" \
"ds_read_b128 v[56:59], %[aph] offset:4096\n" \
"v_mfma_f32_16x16x32_bf16 v[0:3], v[60:63], v[208:211], v[0:3]\n" \
"v_mfma_f32_16x16x32_bf16 v[4:7], v[60:63], v[212:215], v[4:7]\n" \
"v_mfma_f32_16x16x32_bf16 v[8:11], v[64:67], v[208:211], v[8:11]\n" \
"v_mfma_f32_16x16x32_bf16 v[12:15], v[64:67], v[212:215], v[12:15]\n" \
"s_mov_b32 %[so], %[so2]\n" \
"s_add_u32 m0, %[sd], 81920\n" \
"s_nop 0\n" \
"buffer_load_dwordx4 %[a16], s[56:59], %[so] offen sc1 lds\n" \
"v_mfma_f32_16x16x32_bf16 v[16:19], v[68:71], v[208:211], v[16:19]\n" \
"v_mfma_f32_16x16x32_bf16 v[20:23], v[68:71], v[212:215], v[20:23]\n" \
"v_mfma_f32_16x16x32_bf16 v[24:27], v[72:75], v[208:211], v[24:27]\n" \
"v_mfma_f32_16x16x32_bf16 v[28:31], v[72:75], v[212:215], v[28:31]\n" \
"s_add_u32 m0, %[sd], 82944\n" \
"s_add_u32 %[so4], %[so], 1024\n" \
"buffer_load_dwordx4 %[a16], s[56:59], %[so4] offen sc1 lds\n" \
"v_mfma_f32_16x16x32_bf16 v[32:35], v[76:79], v[208:211], v[32:35]\n" \
"v_mfma_f32_16x16x32_bf16 v[36:39], v[76:79], v[212:215], v[36:39]\n" \
"buffer_load_dwordx2 v[148:149], %[a16], s[56:59], %[flo] offen sc1\n" \
"ds_read_b128 v[60:63], %[aph] offset:20480\n" \
"ds_read_b128 v[64:67], %[aph] offset:21504\n" \
"ds_read_b128 v[68:71], %[aph] offset:22528\n" \
"ds_read_b128 v[72:75], %[aph] offset:23552\n" \
"ds_read_b128 v[76:79], %[aph] offset:24576\n" \
"s_waitcnt lgkmcnt(5)\n" \
"v_mfma_f32_16x16x32_bf16 v[0:3], v[40:43], v[216:219], v[0:3]\n" \
"v_mfma_f32_16x16x32_bf16 v[4:7], v[40:43], v[220:223], v[4:7]\n" \
"s_add_u32 m0, %[sd], 83968\n" \
"s_add_u32 %[so4], %[so], 2048\n" \
"buffer_load_dwordx4 %[a16], s[56:59], %[so4] offen sc1 lds\n" \
"v_mfma_f32_16x16x32_bf16 v[8:11], v[44:47], v[216:219], v[8:11]\n" \
"v_mfma_f32_16x16x32_bf16 v[12:15], v[44:47], v[220:223], v[12:15]\n" \
"v_mfma_f32_16x16x32_bf16 v[16:19], v[48:51], v[216:219], v[16:19]\n" \
"v_mfma_f32_16x16x32_bf16 v[20:23], v[48:51], v[220:223], v[20:23]\n" \
"s_add_u32 m0, %[sd], 84992\n" \
"s_add_u32 %[so4], %[so], 3072\n" \
"buffer_load_dwordx4 %[a16], s[56:59], %[so4] offen sc1 lds\n" \
"v_mfma_f32_16x16x32_bf16 v[24:27], v[52:55], v[216:219], v[24:27]\n" \
"v_mfma_f32_16x16x32_bf16 v[28:31], v[52:55], v[220:223], v[28:31]\n" \
"v_mfma_f32_16x16x32_bf16 v[32:35], v[56:59], v[216:219], v[32:35]\n" \
"v_mfma_f32_16x16x32_bf16 v[36:39], v[56:59], v[220:223], v[36:39]\n" \
"s_add_u32 m0, %[sd], 86016\n" \
"s_add_u32 %[so4], %[so], 4096\n" \
"buffer_load_dwordx4 %[a16], s[56:59], %[so4] offen sc1 lds\n" \
"s_waitcnt vmcnt(3) lgkmcnt(0)\n" \
"s_barrier\n" \
"s_mov_b32 %[spin], 0x8000\n" \
"2:\n" \
"v_cmp_eq_u32 vcc, s46, v148\n" \
"v_cmp_eq_u32 %[tmp], s47, v149\n" \
"s_nop 1\n" \
"s_and_b64 vcc, vcc, %[tmp]\n" \
"s_cmp_eq_u64 vcc, -1\n" \
"s_cbranch_scc1 3f\n" \
"s_sub_u32 %[spin], %[spin], 1\n" \
"s_cmp_eq_u32 %[spin], 0\n" \
"s_cbranch_scc1 3f\n" \
"s_sleep 1\n" \
"buffer_load_dwordx2 v[148:149], %[a16], s[56:59], %[flo] offen sc1\n" \
"s_waitcnt vmcnt(0)\n" \
"s_branch 2b\n" \
"3:\n" \
"s_mov_b32 vcc_lo, %[lo4]\n" \
"s_mov_b32 vcc_hi, %[lo4]\n" \
"s_add_u32 %[so4], %[ofo], 0\n" \
"buffer_load_dwordx4 v[80:83], %[a16], s[56:59], %[so4] offen sc1\n" \
"s_add_u32 %[so4], %[ofo], 20480\n" \
"buffer_load_dwordx4 v[84:87], %[a16], s[56:59], %[so4] offen sc1\n" \
"s_add_u32 %[so4], %[ofo], 1024\n" \
"buffer_load_dwordx4 v[88:91], %[a16], s[56:59], %[so4] offen sc1\n" \
"s_add_u32 %[so4], %[ofo], 21504\n" \
"buffer_load_dwordx4 v[92:95], %[a16], s[56:59], %[so4] offen sc1\n" \
"s_add_u32 %[so4], %[ofo], 2048\n" \
"buffer_load_dwordx4 v[96:99], %[a16], s[56:59], %[so4] offen sc1\n" \
"s_add_u32 %[so4], %[ofo], 22528\n" \
"buffer_load_dwordx4 v[100:103], %[a16], s[56:59], %[so4] offen sc1\n" \
"s_add_u32 %[so4], %[ofo], 3072\n" \
"buffer_load_dwordx4 v[104:107], %[a16], s[56:59], %[so4] offen sc1\n" \
"s_add_u32 %[so4], %[ofo], 23552\n" \
"buffer_load_dwordx4 v[108:111], %[a16], s[56:59], %[so4] offen sc1\n" \
"s_add_u32 %[so4], %[ofo], 4096\n" \
"buffer_load_dwordx4 v[112:115], %[a16], s[56:59], %[so4] offen sc1\n" \
"s_add_u32 %[so4], %[ofo], 24576\n" \
"buffer_load_dwordx4 v[116:119], %[a16], s[56:59], %[so4] offen sc1\n" \
"ds_read_b128 v[40:43], %[apl] offset:0\n" \
"ds_read_b128 v[44:47], %[apl] offset:1024\n" \
"ds_read_b128 v[48:51], %[apl] offset:2048\n" \
"ds_read_b128 v[52:55], %[apl] offset:3072\n" \
"ds_read_b128 v[56:59], %[apl] offset:4096\n" \
"v_mfma_f32_16x16x32_bf16 v[0:3], v[60:63], v[224:227], v[0:3]\n" \
"v_mfma_f32_16x16x32_bf16 v[4:7], v[60:63], v[228:231], v[4:7]\n" \
"v_mfma_f32_16x16x32_bf16 v[8:11], v[64:67], v[224:227], v[8:11]\n" \
"v_mfma_f32_16x16x32_bf16 v[12:15], v[64:67], v[228:231], v[12:15]\n" \
"s_add_u32 %[so], %[so], 10240\n" \
"s_add_u32 m0, %[sd], 122880\n" \
"s_nop 0\n" \
"buffer_load_dwordx4 %[a16], s[56:59], %[so] offen sc1 lds\n" \
"v_mfma_f32_16x16x32_bf16 v[16:19], v[68:71], v[224:227], v[16:19]\n" \
"v_mfma_f32_16x16x32_bf16 v[20:23], v[68:71], v[228:231], v[20:23]\n" \
"v_mfma_f32_16x16x32_bf16 v[24:27], v[72:75], v[224:227], v[24:27]\n" \
"v_mfma_f32_16x16x32_bf16 v[28:31], v[72:75], v[228:231], v[28:31]\n" \
"s_add_u32 m0, %[sd], 123904\n" \
"s_add_u32 %[so4], %[so], 1024\n" \
"buffer_load_dwordx4 %[a16], s[56:59], %[so4] offen sc1 lds\n" \
"v_mfma_f32_16x16x32_bf16 v[32:35], v[76:79], v[224:227], v[32:35]\n" \
"v_mfma_f32_16x16x32_bf16 v[36:39], v[76:79], v[228:231], v[36:39]\n" \
"ds_read_b128 v[60:63], %[apl] offset:20480\n" \
"ds_read_b128 v[64:67], %[apl] offset:21504\n" \
"ds_read_b128 v[68:71], %[apl] offset:22528\n" \
"ds_read_b128 v[72:75], %[apl] offset:23552\n" \
"ds_read_b128 v[76:79], %[apl] offset:24576\n" \
"s_waitcnt lgkmcnt(5)\n" \
"v_mfma_f32_16x16x32_bf16 v[0:3], v[40:43], v[232:235], v[0:3]\n" \
"v_mfma_f32_16x16x32_bf16 v[4:7], v[40:43], v[236:239], v[4:7]\n" \
"s_add_u32 m0, %[sd], 124928\n" \
"s_add_u32 %[so4], %[so], 2048\n" \
"buffer_load_dwordx4 %[a16], s[56:59], %[so4] offen sc1 lds\n" \
"v_mfma_f32_16x16x32_bf16 v[8:11], v[44:47], v[232:235], v[8:11]\n" \
"v_mfma_f32_16x16x32_bf16 v[12:15], v[44:47], v[236:239], v[12:15]\n" \
"v_mfma_f32_16x16x32_bf16 v[16:19], v[48:51], v[232:235], v[16:19]\n" \
"v_mfma_f32_16x16x32_bf16 v[20:23], v[48:51], v[236:239], v[20:23]\n" \
"s_add_u32 m0, %[sd], 125952\n" \
"s_add_u32 %[so4], %[so], 3072\n" \
"buffer_load_dwordx4 %[a16], s[56:59], %[so4] offen sc1 lds\n" \
"v_mfma_f32_16x16x32_bf16 v[24:27], v[52:55], v[232:235], v[24:27]\n" \
"v_mfma_f32_16x16x32_bf16 v[28:31], v[52:55], v[236:239], v[28:31]\n" \
"v_mfma_f32_16x16x32_bf16 v[32:35], v[56:59], v[232:235], v[32:35]\n" \
"v_mfma_f32_16x16x32_bf16 v[36:39], v[56:59], v[236:239], v[36:39]\n" \
"s_add_u32 m0, %[sd], 126976\n" \
"s_add_u32 %[so4], %[so], 4096\n" \
"buffer_load_dwordx4 %[a16], s[56:59], %[so4] offen sc1 lds\n" \
"s_waitcnt lgkmcnt(0)\n" \
"s_barrier\n" \
"ds_read_b128 v[40:43], %[apl] offset:40960\n" \
"ds_read_b128 v[44:47], %[apl] offset:41984\n" \
"ds_read_b128 v[48:51], %[apl] offset:43008\n" \
"ds_read_b128 v[52:55], %[apl] offset:44032\n" \
"ds_read_b128 v[56:59], %[apl] offset:45056\n" \
"v_mfma_f32_16x16x32_bf16 v[0:3], v[60:63], v[240:243], v[0:3]\n" \
"v_mfma_f32_16x16x32_bf16 v[4:7], v[60:63], v[244:247], v[4:7]\n" \
"v_mfma_f32_16x16x32_bf16 v[8:11], v[64:67], v[240:243], v[8:11]\n" \
"v_mfma_f32_16x16x32_bf16 v[12:15], v[64:67], v[244:247], v[12:15]\n" \
"s_add_u32 %[so], %[so], 10240\n" \
"s_add_u32 m0, %[sd], 0\n" \
"s_nop 0\n" \
"s_cbranch_vccz 80f\n" \
"buffer_load_dwordx4 %[a16], s[56:59], %[so] offen sc1 lds\n" \
"80:\n" \
"v_mfma_f32_16x16x32_bf16 v[16:19], v[68:71], v[240:243], v[16:19]\n" \
"v_mfma_f32_16x16x32_bf16 v[20:23], v[68:71], v[244:247], v[20:23]\n" \
"v_mfma_f32_16x16x32_bf16 v[24:27], v[72:75], v[240:243], v[24:27]\n" \
"v_mfma_f32_16x16x32_bf16 v[28:31], v[72:75], v[244:247], v[28:31]\n" \
"s_add_u32 m0, %[sd], 1024\n" \
"s_add_u32 %[so4], %[so], 1024\n" \
"s_cbranch_vccz 81f\n" \
"buffer_load_dwordx4 %[a16], s[56:59], %[so4] offen sc1 lds\n" \
"81:\n" \
"v_mfma_f32_16x16x32_bf16 v[32:35], v[76:79], v[240:243], v[32:35]\n" \
"v_mfma_f32_16x16x32_bf16 v[36:39], v[76:79], v[244:247], v[36:39]\n" \
"buffer_load_dwordx4 v[120:123], %[bo0], %[b2r], 0 offen\n" \
"s_cmp_ge_u32 %[wid], 2\n" \
"s_cbranch_scc1 9f\n" \
"buffer_load_dwordx4 v[124:127], %[bo1], %[b2r], 0 offen\n" \
"9:\n" \
"ds_read_b128 v[60:63], %[apm] offset:0\n" \
"ds_read_b128 v[64:67], %[apm] offset:1024\n" \
"ds_read_b128 v[68:71], %[apm] offset:2048\n" \
"ds_read_b128 v[72:75], %[apm] offset:3072\n" \
"ds_read_b128 v[76:79], %[apm] offset:4096\n" \
"s_waitcnt lgkmcnt(5)\n" \
"v_mfma_f32_16x16x32_bf16 v[0:3], v[40:43], v[248:251], v[0:3]\n" \
"v_mfma_f32_16x16x32_bf16 v[4:7], v[40:43], v[252:255], v[4:7]\n" \
"s_add_u32 m0, %[sd], 2048\n" \
"s_add_u32 %[so4], %[so], 2048\n" \
"s_cbranch_vccz 82f\n" \
"buffer_load_dwordx4 %[a16], s[56:59], %[so4] offen sc1 lds\n" \
"82:\n" \
"v_mfma_f32_16x16x32_bf16 v[8:11], v[44:47], v[248:251], v[8:11]\n" \
"v_mfma_f32_16x16x32_bf16 v[12:15], v[44:47], v[252:255], v[12:15]\n" \
"v_mfma_f32_16x16x32_bf16 v[16:19], v[48:51], v[248:251], v[16:19]\n" \
"v_mfma_f32_16x16x32_bf16 v[20:23], v[48:51], v[252:255], v[20:23]\n" \
"s_add_u32 m0, %[sd], 3072\n" \
"s_add_u32 %[so4], %[so], 3072\n" \
"s_cbranch_vccz 83f\n" \
"buffer_load_dwordx4 %[a16], s[56:59], %[so4] offen sc1 lds\n" \
"83:\n" \
"v_mfma_f32_16x16x32_bf16 v[24:27], v[52:55], v[248:251], v[24:27]\n" \
"v_mfma_f32_16x16x32_bf16 v[28:31], v[52:55], v[252:255], v[28:31]\n" \
"v_mfma_f32_16x16x32_bf16 v[32:35], v[56:59], v[248:251], v[32:35]\n" \
"v_mfma_f32_16x16x32_bf16 v[36:39], v[56:59], v[252:255], v[36:39]\n" \
"s_add_u32 m0, %[sd], 4096\n" \
"s_add_u32 %[so4], %[so], 4096\n" \
"s_cbranch_vccz 84f\n" \
"buffer_load_dwordx4 %[a16], s[56:59], %[so4] offen sc1 lds\n" \
"84:\n" \
"s_waitcnt vmcnt(6) lgkmcnt(0)\n" \
"s_barrier\n" \
"ds_read_b128 v[40:43], %[apm] offset:20480\n" \
"ds_read_b128 v[44:47], %[apm] offset:21504\n" \
"ds_read_b128 v[48:51], %[apm] offset:22528\n" \
"ds_read_b128 v[52:55], %[apm] offset:23552\n" \
"ds_read_b128 v[56:59], %[apm] offset:24576\n" \
"v_mfma_f32_16x16x32_bf16 v[0:3], v[60:63], v[128:131], v[0:3]\n" \
"v_mfma_f32_16x16x32_bf16 v[4:7], v[60:63], v[134:137], v[4:7]\n" \
"v_mfma_f32_16x16x32_bf16 v[8:11], v[64:67], v[128:131], v[8:11]\n" \
"v_mfma_f32_16x16x32_bf16 v[12:15], v[64:67], v[134:137], v[12:15]\n" \
"v_mfma_f32_16x16x32_bf16 v[16:19], v[68:71], v[128:131], v[16:19]\n" \
"v_mfma_f32_16x16x32_bf16 v[20:23], v[68:71], v[134:137], v[20:23]\n" \
"v_mfma_f32_16x16x32_bf16 v[24:27], v[72:75], v[128:131], v[24:27]\n" \
"v_mfma_f32_16x16x32_bf16 v[28:31], v[72:75], v[134:137], v[28:31]\n" \
"v_mfma_f32_16x16x32_bf16 v[32:35], v[76:79], v[128:131], v[32:35]\n" \
"v_mfma_f32_16x16x32_bf16 v[36:39], v[76:79], v[134:137], v[36:39]\n" \
"ds_read_b128 v[60:63], %[apm] offset:40960\n" \
"ds_read_b128 v[64:67], %[apm] offset:41984\n" \
"ds_read_b128 v[68:71], %[apm] offset:43008\n" \
"ds_read_b128 v[72:75], %[apm] offset:44032\n" \
"ds_read_b128 v[76:79], %[apm] offset:45056\n" \
"s_waitcnt lgkmcnt(5)\n" \
"v_mfma_f32_16x16x32_bf16 v[0:3], v[40:43], v[80:83], v[0:3]\n" \
"v_mfma_f32_16x16x32_bf16 v[4:7], v[40:43], v[84:87], v[4:7]\n" \
"v_mfma_f32_16x16x32_bf16 v[8:11], v[44:47], v[80:83], v[8:11]\n" \
"v_mfma_f32_16x16x32_bf16 v[12:15], v[44:47], v[84:87], v[12:15]\n" \
"v_mfma_f32_16x16x32_bf16 v[16:19], v[48:51], v[80:83], v[16:19]\n" \
"v_mfma_f32_16x16x32_bf16 v[20:23], v[48:51], v[84:87], v[20:23]\n" \
"v_mfma_f32_16x16x32_bf16 v[24:27], v[52:55], v[80:83], v[24:27]\n" \
"v_mfma_f32_16x16x32_bf16 v[28:31], v[52:55], v[84:87], v[28:31]\n" \
"v_mfma_f32_16x16x32_bf16 v[32:35], v[56:59], v[80:83], v[32:35]\n" \
"v_mfma_f32_16x16x32_bf16 v[36:39], v[56:59], v[84:87], v[36:39]\n" \
"s_waitcnt vmcnt(1) lgkmcnt(0)\n" \
"s_barrier\n" \
"ds_read_b128 v[40:43], %[aph] offset:0\n" \
"ds_read_b128 v[44:47], %[aph] offset:1024\n" \
"ds_read_b128 v[48:51], %[aph] offset:2048\n" \
"ds_read_b128 v[52:55], %[aph] offset:3072\n" \
"ds_read_b128 v[56:59], %[aph] offset:4096\n" \
"v_mfma_f32_16x16x32_bf16 v[0:3], v[60:63], v[88:91], v[0:3]\n" \
"v_mfma_f32_16x16x32_bf16 v[4:7], v[60:63], v[92:95], v[4:7]\n" \
"v_mfma_f32_16x16x32_bf16 v[8:11], v[64:67], v[88:91], v[8:11]\n" \
"v_mfma_f32_16x16x32_bf16 v[12:15], v[64:67], v[92:95], v[12:15]\n" \
"v_mfma_f32_16x16x32_bf16 v[16:19], v[68:71], v[88:91], v[16:19]\n" \
"v_mfma_f32_16x16x32_bf16 v[20:23], v[68:71], v[92:95], v[20:23]\n" \
"v_mfma_f32_16x16x32_bf16 v[24:27], v[72:75], v[88:91], v[24:27]\n" \
"v_mfma_f32_16x16x32_bf16 v[28:31], v[72:75], v[92:95], v[28:31]\n" \
"v_mfma_f32_16x16x32_bf16 v[32:35], v[76:79], v[88:91], v[32:35]\n" \
"v_mfma_f32_16x16x32_bf16 v[36:39], v[76:79], v[92:95], v[36:39]\n" \
"ds_read_b128 v[60:63], %[aph] offset:20480\n" \
"ds_read_b128 v[64:67], %[aph] offset:21504\n" \
"ds_read_b128 v[68:71], %[aph] offset:22528\n" \
"ds_read_b128 v[72:75], %[aph] offset:23552\n" \
"ds_read_b128 v[76:79], %[aph] offset:24576\n" \
"s_waitcnt lgkmcnt(5)\n" \
"v_mfma_f32_16x16x32_bf16 v[0:3], v[40:43], v[96:99], v[0:3]\n" \
"v_mfma_f32_16x16x32_bf16 v[4:7], v[40:43], v[100:103], v[4:7]\n" \
"v_mfma_f32_16x16x32_bf16 v[8:11], v[44:47], v[96:99], v[8:11]\n" \
"v_mfma_f32_16x16x32_bf16 v[12:15], v[44:47], v[100:103], v[12:15]\n" \
"v_mfma_f32_16x16x32_bf16 v[16:19], v[48:51], v[96:99], v[16:19]\n" \
"v_mfma_f32_16x16x32_bf16 v[20:23], v[48:51], v[100:103], v[20:23]\n" \
"v_mfma_f32_16x16x32_bf16 v[24:27], v[52:55], v[96:99], v[24:27]\n" \
"v_mfma_f32_16x16x32_bf16 v[28:31], v[52:55], v[100:103], v[28:31]\n" \
"v_mfma_f32_16x16x32_bf16 v[32:35], v[56:59], v[96:99], v[32:35]\n" \
"v_mfma_f32_16x16x32_bf16 v[36:39], v[56:59], v[100:103], v[36:39]\n" \
"s_waitcnt vmcnt(0) lgkmcnt(0)\n" \
"s_barrier\n" \
"ds_read_b128 v[40:43], %[apl] offset:0\n" \
"ds_read_b128 v[44:47], %[apl] offset:1024\n" \
"ds_read_b128 v[48:51], %[apl] offset:2048\n" \
"ds_read_b128 v[52:55], %[apl] offset:3072\n" \
"ds_read_b128 v[56:59], %[apl] offset:4096\n" \
"v_mfma_f32_16x16x32_bf16 v[0:3], v[60:63], v[104:107], v[0:3]\n" \
"v_mfma_f32_16x16x32_bf16 v[4:7], v[60:63], v[108:111], v[4:7]\n" \
"v_mfma_f32_16x16x32_bf16 v[8:11], v[64:67], v[104:107], v[8:11]\n" \
"v_mfma_f32_16x16x32_bf16 v[12:15], v[64:67], v[108:111], v[12:15]\n" \
"v_mfma_f32_16x16x32_bf16 v[16:19], v[68:71], v[104:107], v[16:19]\n" \
"v_mfma_f32_16x16x32_bf16 v[20:23], v[68:71], v[108:111], v[20:23]\n" \
"v_mfma_f32_16x16x32_bf16 v[24:27], v[72:75], v[104:107], v[24:27]\n" \
"v_mfma_f32_16x16x32_bf16 v[28:31], v[72:75], v[108:111], v[28:31]\n" \
"v_mfma_f32_16x16x32_bf16 v[32:35], v[76:79], v[104:107], v[32:35]\n" \
"v_mfma_f32_16x16x32_bf16 v[36:39], v[76:79], v[108:111], v[36:39]\n" \
"s_waitcnt lgkmcnt(0)\n" \
"v_mfma_f32_16x16x32_bf16 v[0:3], v[40:43], v[112:115], v[0:3]\n" \
"v_mfma_f32_16x16x32_bf16 v[4:7], v[40:43], v[116:119], v[4:7]\n" \
"v_mfma_f32_16x16x32_bf16 v[8:11], v[44:47], v[112:115], v[8:11]\n" \
"v_mfma_f32_16x16x32_bf16 v[12:15], v[44:47], v[116:119], v[12:15]\n" \
"v_mfma_f32_16x16x32_bf16 v[16:19], v[48:51], v[112:115], v[16:19]\n" \
"v_mfma_f32_16x16x32_bf16 v[20:23], v[48:51], v[116:119], v[20:23]\n" \
"v_mfma_f32_16x16x32_bf16 v[24:27], v[52:55], v[112:115], v[24:27]\n" \
"v_mfma_f32_16x16x32_bf16 v[28:31], v[52:55], v[116:119], v[28:31]\n" \
"v_mfma_f32_16x16x32_bf16 v[32:35], v[56:59], v[112:115], v[32:35]\n" \
"v_mfma_f32_16x16x32_bf16 v[36:39], v[56:59], v[116:119], v[36:39]\n" \
"s_waitcnt vmcnt(0)\n" \
"s_mov_b32 m0, %[keep]\n" \
"s_nop 7\n" \
"s_nop 7\n"

#define LN_STATS \
"s_waitcnt vmcnt(12)\n" \
"v_dot2_f32_bf16 %[s10], v176, %[ones], 0\n" \
"v_dot2_f32_bf16 %[s20], v176, v176, 0\n" \
"v_dot2_f32_bf16 %[s11], v177, %[ones], 0\n" \
"v_dot2_f32_bf16 %[s21], v177, v177, 0\n" \
"v_dot2_f32_bf16 %[s10], v178, %[ones], %[s10]\n" \
"v_dot2_f32_bf16 %[s20], v178, v178, %[s20]\n" \
"v_dot2_f32_bf16 %[s11], v179, %[ones], %[s11]\n" \
"v_dot2_f32_bf16 %[s21], v179, v179, %[s21]\n" \
"s_waitcnt vmcnt(11)\n" \
"v_dot2_f32_bf16 %[s10], v180, %[ones], %[s10]\n" \
"v_dot2_f32_bf16 %[s20], v180, v180, %[s20]\n" \
"v_dot2_f32_bf16 %[s11], v181, %[ones], %[s11]\n" \
"v_dot2_f32_bf16 %[s21], v181, v181, %[s21]\n" \
"v_dot2_f32_bf16 %[s10], v182, %[ones], %[s10]\n" \
"v_dot2_f32_bf16 %[s20], v182, v182, %[s20]\n" \
"v_dot2_f32_bf16 %[s11], v183, %[ones], %[s11]\n" \
"v_dot2_f32_bf16 %[s21], v183, v183, %[s21]\n" \
"s_waitcnt vmcnt(10)\n" \
"v_dot2_f32_bf16 %[s10], v184, %[ones], %[s10]\n" \
"v_dot2_f32_bf16 %[s20], v184, v184, %[s20]\n" \
"v_dot2_f32_bf16 %[s11], v185, %[ones], %[s11]\n" \
"v_dot2_f32_bf16 %[s21], v185, v185, %[s21]\n" \
"v_dot2_f32_bf16 %[s10], v186, %[ones], %[s10]\n" \
"v_dot2_f32_bf16 %[s20], v186, v186, %[s20]\n" \
"v_dot2_f32_bf16 %[s11], v187, %[ones], %[s11]\n" \
"v_dot2_f32_bf16 %[s21], v187, v187, %[s21]\n" \
"s_waitcnt vmcnt(9)\n" \
"v_dot2_f32_bf16 %[s10], v188, %[ones], %[s10]\n" \
"v_dot2_f32_bf16 %[s20], v188, v188, %[s20]\n" \
"v_dot2_f32_bf16 %[s11], v189, %[ones], %[s11]\n" \
"v_dot2_f32_bf16 %[s21], v189, v189, %[s21]\n" \
"v_dot2_f32_bf16 %[s10], v190, %[ones], %[s10]\n" \
"v_dot2_f32_bf16 %[s20], v190, v190, %[s20]\n" \
"v_dot2_f32_bf16 %[s11], v191, %[ones], %[s11]\n" \
"v_dot2_f32_bf16 %[s21], v191, v191, %[s21]\n" \
"s_waitcnt vmcnt(8)\n" \
"v_dot2_f32_bf16 %[s10], v192, %[ones], %[s10]\n" \
"v_dot2_f32_bf16 %[s20], v192, v192, %[s20]\n" \
"v_dot2_f32_bf16 %[s11], v193, %[ones], %[s11]\n" \
"v_dot2_f32_bf16 %[s21], v193, v193, %[s21]\n" \
"v_dot2_f32_bf16 %[s10], v194, %[ones], %[s10]\n" \
"v_dot2_f32_bf16 %[s20], v194, v194, %[s20]\n" \
"v_dot2_f32_bf16 %[s11], v195, %[ones], %[s11]\n" \
"v_dot2_f32_bf16 %[s21], v195, v195, %[s21]\n" \
"s_waitcnt vmcnt(7)\n" \
"v_dot2_f32_bf16 %[s10], v196, %[ones], %[s10]\n" \
"v_dot2_f32_bf16 %[s20], v196, v196, %[s20]\n" \
"v_dot2_f32_bf16 %[s11], v197, %[ones], %[s11]\n" \
"v_dot2_f32_bf16 %[s21], v197, v197, %[s21]\n" \
"v_dot2_f32_bf16 %[s10], v198, %[ones], %[s10]\n" \
"v_dot2_f32_bf16 %[s20], v198, v198, %[s20]\n" \
"v_dot2_f32_bf16 %[s11], v199, %[ones], %[s11]\n" \
"v_dot2_f32_bf16 %[s21], v199, v199, %[s21]\n" \
"s_waitcnt vmcnt(6)\n" \
"v_dot2_f32_bf16 %[s10], v200, %[ones], %[s10]\n" \
"v_dot2_f32_bf16 %[s20], v200, v200, %[s20]\n" \
"v_dot2_f32_bf16 %[s11], v201, %[ones], %[s11]\n" \
"v_dot2_f32_bf16 %[s21], v201, v201, %[s21]\n" \
"v_dot2_f32_bf16 %[s10], v202, %[ones], %[s10]\n" \
"v_dot2_f32_bf16 %[s20], v202, v202, %[s20]\n" \
"v_dot2_f32_bf16 %[s11], v203, %[ones], %[s11]\n" \
"v_dot2_f32_bf16 %[s21], v203, v203, %[s21]\n" \
"s_waitcnt vmcnt(5)\n" \
"v_dot2_f32_bf16 %[s10], v204, %[ones], %[s10]\n" \
"v_dot2_f32_bf16 %[s20], v204, v204, %[s20]\n" \
"v_dot2_f32_bf16 %[s11], v205, %[ones], %[s11]\n" \
"v_dot2_f32_bf16 %[s21], v205, v205, %[s21]\n" \
"v_dot2_f32_bf16 %[s10], v206, %[ones], %[s10]\n" \
"v_dot2_f32_bf16 %[s20], v206, v206, %[s20]\n" \
"v_dot2_f32_bf16 %[s11], v207, %[ones], %[s11]\n" \
"v_dot2_f32_bf16 %[s21], v207, v207, %[s21]\n" \
"s_waitcnt vmcnt(4)\n" \
"v_dot2_f32_bf16 %[s10], v208, %[ones], %[s10]\n" \
"v_dot2_f32_bf16 %[s20], v208, v208, %[s20]\n" \
"v_dot2_f32_bf16 %[s11], v209, %[ones], %[s11]\n" \
"v_dot2_f32_bf16 %[s21], v209, v209, %[s21]\n" \
"v_dot2_f32_bf16 %[s10], v210, %[ones], %[s10]\n" \
"v_dot2_f32_bf16 %[s20], v210, v210, %[s20]\n" \
"v_dot2_f32_bf16 %[s11], v211, %[ones], %[s11]\n" \
"v_dot2_f32_bf16 %[s21], v211, v211, %[s21]\n" \
"s_waitcnt vmcnt(3)\n" \
"v_dot2_f32_bf16 %[s10], v212, %[ones], %[s10]\n" \
"v_dot2_f32_bf16 %[s20], v212, v212, %[s20]\n" \
"v_dot2_f32_bf16 %[s11], v213, %[ones], %[s11]\n" \
"v_dot2_f32_bf16 %[s21], v213, v213, %[s21]\n" \
"v_dot2_f32_bf16 %[s10], v214, %[ones], %[s10]\n" \
"v_dot2_f32_bf16 %[s20], v214, v214, %[s20]\n" \
"v_dot2_f32_bf16 %[s11], v215, %[ones], %[s11]\n" \
"v_dot2_f32_bf16 %[s21], v215, v215, %[s21]\n" \
"s_nop 2\n"

#define X_LOADS \
"buffer_load_dwordx4 v[176:179], %[xo], %[xrs], 0 offen offset:0\n" \
"buffer_load_dwordx4 v[180:183], %[xo], %[xrs], 0 offen offset:128\n" \
"buffer_load_dwordx4 v[184:187], %[xo], %[xrs], 0 offen offset:256\n" \
"buffer_load_dwordx4 v[188:191], %[xo], %[xrs], 0 offen offset:384\n" \
"buffer_load_dwordx4 v[192:195], %[xo], %[xrs], 0 offen offset:512\n" \
"buffer_load_dwordx4 v[196:199], %[xo], %[xrs], 0 offen offset:640\n" \
"buffer_load_dwordx4 v[200:203], %[xo], %[xrs], 0 offen offset:768\n" \
"buffer_load_dwordx4 v[204:207], %[xo], %[xrs], 0 offen offset:896\n" \
"buffer_load_dwordx4 v[208:211], %[xo], %[xrs], 0 offen offset:1024\n" \
"buffer_load_dwordx4 v[212:215], %[xo], %[xrs], 0 offen offset:1152\n"

__global__ __launch_bounds__(512) void fused_block(const u16* __restrict__ x, const u16* __restrict__ wq, const u16* __restrict__ wk, const u16* __restrict__ wv,
                                                   const u16* __restrict__ wo, const u16* __restrict__ w1, const u16* __restrict__ b1, const u16* __restrict__ w2,
                                                   const u16* __restrict__ b2, u16* __restrict__ out, char* __restrict__ ws) {
  extern __shared__ __attribute__((aligned(16))) char lds_raw[];
  const lptr lds = (lptr)lds_raw;
  const unsigned lds0 = (unsigned)(uintptr_t)lds_raw;
  const int tid = threadIdx.x, lane = tid & 63, r = lane & 31, h = lane >> 5;
  const int w = __builtin_amdgcn_readfirstlane(tid >> 6);
  const int b = blockIdx.x;
  const int tb = (b & 1) * 16 + (b >> 4), g = ((b >> 1) & 3) * 2 + ((b >> 3) & 1); constexpr int SHARE = 16; const int sh = b >> 4;
  const rsrc_t wsr = __builtin_amdgcn_make_buffer_rsrc((void*)ws, 0, (int)WS_END, 0x00020000);
#define STAMP(i) do {} while (0)
#define STAMP_OUT(n) do {} while (0)
#define STAMP_OUT2(a, n) do {} while (0)
#define ASM_ST_CLOB
#define ASM_ST_OUT(which) do {} while (0)
  STAMP(0);
  if (w == 5) {
    u64 pc; asm volatile("s_getpc_b64 %0" : "=s"(pc));
    constexpr int NL = (CODE_BYTES / 128 + 31) / 32;
    const int l = (b >> 3) * NL + lane;
    bdma4(MKRS(pc, CODE_BYTES), lane < NL ? (unsigned)l * 128u : 0x7fffffffu, lds0 + MISC + 6144);
  }
  u32x4 b1v; u32x16 X0, X1; u32x8 X2;
  u64 epoch;
  {
    const unsigned xo = (unsigned)(tb * 64 + 8 * w + (lane >> 3)) * 1280 + (lane & 7) * 16;
    asm volatile(X_LOADS : "=&{v[176:191]}"(X0), "=&{v[192:207]}"(X1), "=&{v[208:215]}"(X2) : [xo] "v"(xo), [xrs] "s"(MKRS(x, 2621440)));
    asm volatile("s_load_dwordx2 %0, %1, 0x0" : "=s"(epoch) : "s"(ws));
    asm volatile("s_barrier" ::: "memory");
  }
  const char* srcA[3]; u64 strA[3]; unsigned offF[3];
  const rsrc_t w1r = MKRS(w1, 1966080);
#pragma unroll
  for (int u = 0; u < 3; ++u) {
    const int L = 64 * (3 * w + u) + lane;
    { const int k = L / 12, p = L % 12;
      const int gk = g < 6 ? g : 0; const u16* kvm = gk < 3 ? wk : wv; const int ko = (gk % 3 == 2) ? 24 : (gk % 3) * 16;
      strA[u] = p < 10 ? 163840 : 5120;
      srcA[u] = (p < 10 ? (const char*)wq + k * 1280 + 160 * g + 16 * p : (const char*)kvm + k * 40 + ko) - strA[u]; }
    { const int k = L / 24, s = L % 24, p = (((s >> 2) ^ ((k >> 1) & 1)) << 2) | (s & 3);
      offF[u] = k * 3072 + 384 * g + 16 * p; }
  }
  auto issue_chunk = [&](int cc) {
    const unsigned dst = lds0 + RING + (cc % 3) * SLOT + w * 3072;
    if (cc < 5) {
#pragma unroll
      for (int u = 0; u < 3; ++u) glds16_pre(srcA[u], strA[u], dst + u * 1024);
    } else if (cc < 15) {
#pragma unroll
      for (int u = 0; u < 3; ++u) bdma16(w1r, offF[u], (cc - 5) * 196608, dst + u * 1024);
    }
  };
  float ln_rs, ln_c0;
  {
    float s10, s11, s20, s21;
    issue_chunk(0);
    {
      constexpr int NQ = 1280 / SHARE, NF = 1920 / SHARE;
#pragma unroll
      for (int i = 0; i < (NQ + 511) / 512; ++i) { const int li = i * 512 + w * 64 + lane, l = sh * NQ + li;
        if (i * 512 + w * 64 < NQ) bdma4(MKRS(wq, 819200), li < NQ ? (unsigned)((l >> 1) * 1280 + ((160 * g) & ~127) + (l & 1) * 128) : 0x7fffffffu, lds0 + MISC + 6144); }
#pragma unroll
      for (int i = 0; i < (NF + 511) / 512; ++i) { const int li = i * 512 + (7 - w) * 64 + lane, l = sh * NF + li;
        if (i * 512 + (7 - w) * 64 < NF) bdma4(w1r, li < NF ? (unsigned)((l / 3) * 3072 + 384 * g + (l % 3) * 128) : 0x7fffffffu, lds0 + MISC + 6144); }
    }
    if (w >= 4 && w < 7 && tb + 32 * (w - 4) < 68) { const int ks = tb + 32 * (w - 4);
      bdma4(ks < 48 ? MKRS(w2, 1966080) : MKRS(wo, 819200), (unsigned)(((ks < 48 ? ks : ks - 48) * 32 + (lane >> 1)) * 1280 + ((160 * g) & ~127) + (lane & 1) * 128), lds0 + MISC + 6144); }
    asm volatile("s_cmp_lg_u32 %1, 7\n\ts_cbranch_scc1 1f\n\tbuffer_load_dwordx4 %0, %2, %3, 0 offen\n1:" : "=v"(b1v) : "s"(w), "v"(384 * g + min(lane, 23) * 16), "s"(MKRS(b1, 3072)) : "scc");
    asm volatile(LN_STATS : [s10] "=&v"(s10), [s11] "=&v"(s11), [s20] "=&v"(s20), [s21] "=&v"(s21), "+{v[176:191]}"(X0), "+{v[192:207]}"(X1), "+{v[208:215]}"(X2) : [ones] "s"(0x3f803f80u));
    float s1 = sum8(s10 + s11), s2 = sum8(s20 + s21);
    const float m1 = s1 * (1.0f / 640.0f), e2 = s2 * (1.0f / 640.0f);
    float var = fmaxf(e2 - m1 * m1, 0.f);
    if (__builtin_expect(__any(e2 > 4096.f * var), 0)) {
      float t = 0.f;
#pragma unroll
      for (int j = 0; j < 40; ++j) { const unsigned u = j < 16 ? X0[j & 15] : j < 32 ? X1[j & 15] : X2[j & 7]; const float a = bf_lo(u) - m1, c = bf_hi(u) - m1; t = fmaf(a, a, fmaf(c, c, t)); }
      var = sum8(t) * (1.0f / 640.0f);
    }
    const float mb = bf_round(m1);
    const float sd = bf_round(sqrtf(bf_round(bf_round(var) + 1e-5f)));
    ln_rs = 1.0f / sd; ln_c0 = -mb * ln_rs;
  }
  asm volatile("s_waitcnt lgkmcnt(0)" : "+s"(epoch));
  const u64 tag = epoch + 1;
  const u32x2 tagv = {(unsigned)tag, (unsigned)(tag >> 32)};
  STAMP(1);

  u32x16 QAv; u32x8 QBv;
  {
    const int hh = w & 3, tt = w >> 2;
    const int blk = (lane >> 4) & 1, q4 = (lane & 15) >> 2, pp = lane & 3;
    const int colb = blk == 0 ? 40 * hh + 8 * pp : ((pp >= 2 && hh == 0) ? 160 + 8 * (pp - 2) : 40 * hh + 32);
    f32x16 acc;
    { unsigned km0, sf;
      asm volatile(A1_ASM
        : [acc0] "=&v"(acc), [keep] "=&s"(km0), [sf] "=&s"(sf), [pa0] "+v"(srcA[0]), [pa1] "+v"(srcA[1]), [pa2] "+v"(srcA[2])
        : [va] "v"(lds0 + RING + (8 * h + q4) * 192 + colb), [va2] "v"(lds0 + RING + 2 * SLOT + (8 * h + q4) * 192 + colb), [vb] "v"(lds0 + (32 * tt + r) * XS_STR + h * 16),
          [st0] "v"(strA[0]), [st1] "v"(strA[1]), [st2] "v"(strA[2]),
          [of0] "v"(offF[0]), [of1] "v"(offF[1]), [of2] "v"(offF[2]), [w1r] "s"(w1r), [sd] "s"(lds0 + RING + w * 3072),
          [rc] "v"(f32x2{ln_rs, ln_c0}), [xw] "v"(lds0 + (8 * w + (lane >> 3)) * XS_STR + (lane & 7) * 16),
          "{v[176:191]}"(X0), "{v[192:207]}"(X1), "{v[208:215]}"(X2)
        : "v64", "v65", "v66", "v67", "v68", "v69", "v70", "v71", "v72", "v73", "v74", "v75", "v76", "v77", "v78", "v79", "v80", "v81", "v82", "v83", "v84", "v85", "v86", "v87", "v88", "v89", "v90", "v91", "v92", "v93", "v94", "v95", "v216", "v217", "v218", "v219", "v220", "v221", "v222", "v223", "v224", "v225", "v226", "v227", "scc", "memory" ASM_ST_CLOB); ASM_ST_OUT(1); }
    STAMP(2);
    const u32x4 QA = {cvtpk(acc[0] * CSC, acc[1] * CSC), cvtpk(acc[2] * CSC, acc[3] * CSC), cvtpk(acc[4] * CSC, acc[5] * CSC), cvtpk(acc[6] * CSC, acc[7] * CSC)};
    u32x2 QB = {cvtpk(acc[8] * CSC, acc[9] * CSC), cvtpk(acc[10] * CSC, acc[11] * CSC)};
    if (h) { QB.x = 0; QB.y = 0; }
    const u32x2 pk = {cvtpk(acc[12], acc[13]), cvtpk(acc[14], acc[15])};
    BAR();
    asm volatile("" : "+v"(b1v));
    if (w == 7 && lane < 24) *(LAS u32x4*)(lds + MISC + 5120 + lane * 16) = b1v;
    { lptr qd = lds + RING + SLOT + w * 1536; *(LAS u32x4*)(qd + lane * 16) = QA; *(LAS u32x2*)(qd + 1024 + lane * 8) = QB; }
    if (hh == 0 && g < 6) {
      const unsigned tbase = WS_KV + (2 * tb + tt) * KV_TILE;
      if (g < 2) ST8(pk, wsr, tbase + lane * 16 + 8 * g);
      else if (g == 2) { const u32x2 one = {0x3f803f80u, 0x00003f80u}; ST8(h ? pk : one, wsr, tbase + 3072 + (h ? r : 32 + r) * 16); }
      else {
        lptr vt = lds + MISC + 2048 + tt * 512;
        *(LAS u16*)(vt + (4 * h + 0) * 64 + r * 2) = (u16)pk.x; *(LAS u16*)(vt + (4 * h + 1) * 64 + r * 2) = (u16)(pk.x >> 16);
        *(LAS u16*)(vt + (4 * h + 2) * 64 + r * 2) = (u16)pk.y; *(LAS u16*)(vt + (4 * h + 3) * 64 + r * 2) = (u16)(pk.y >> 16);
        asm volatile("s_waitcnt lgkmcnt(0)" ::: "memory");
        const int dl = lane & 7, h2 = (lane >> 3) & 1, s2 = (lane >> 4) & 1;
        const u32x2 a = *(LAS u32x2*)(vt + dl * 64 + (16 * s2 + 4 * h2) * 2), c2 = *(LAS u32x2*)(vt + dl * 64 + (16 * s2 + 8 + 4 * h2) * 2);
        const int d = (g == 5 ? 12 : 8 * (g - 3)) + dl;
        if (lane < 32 && (g != 5 || dl >= 4)) ST16((u32x4{a.x, a.y, c2.x, c2.y}), wsr, tbase + 1024 + s2 * 1024 + (d + 32 * h2) * 16);
        if (g == 5) {
          const int l4 = lane >> 2, dd = 20 + (lane & 3) + 4 * (l4 % 3), hs = l4 / 3; const unsigned cv = dd == 20 ? 0x3f803f80u : 0u;
          if (lane < 48) ST16((u32x4{cv, cv, cv, cv}), wsr, tbase + 1024 + (hs >> 1) * 1024 + (dd + 32 * (hs & 1)) * 16); }
      }
    }
    BAR();
#pragma unroll
    for (int i = 0; i < 4; ++i) { lptr p = lds + RING + SLOT + (((i + w) & 3) + (w & 4)) * 1536;
      const u32x4 t = *(LAS u32x4*)(p + lane * 16); const u32x2 t2 = *(LAS u32x2*)(p + 1024 + lane * 8);
      QAv[4 * i] = t[0]; QAv[4 * i + 1] = t[1]; QAv[4 * i + 2] = t[2]; QAv[4 * i + 3] = t[3]; QBv[2 * i] = t2.x; QBv[2 * i + 1] = t2.y; }
    asm volatile("" : "+{v[104:119]}"(QAv), "+{v[120:127]}"(QBv));
  }
  STAMP(3);
  if (STOP_AFTER <= 1) { STAMP_OUT(4); return; }

  u32x16 kv0; u32x4 kv1; unsigned kvo = WS_KV + (w & 3) * 16 * KV_TILE;
  {
    const bool P = w < 4;
    const int phi = P ? w : 4 + (w & 1), tt = P ? 0 : (w >> 1) & 1;
    const int blk = (lane >> 4) & 1, q4 = (lane & 15) >> 2, pp = lane & 3, sw = (q4 >> 1) & 1;
    f32x16 bias, acc0, acc1;
#pragma unroll
    for (int a = 0; a < 4; ++a) { const u32x2 bb = *(LAS u32x2*)(lds + MISC + 5120 + (32 * phi + 8 * a + 4 * h) * 2);
      bias[4 * a] = bf_lo(bb.x); bias[4 * a + 1] = bf_hi(bb.x); bias[4 * a + 2] = bf_lo(bb.y); bias[4 * a + 3] = bf_hi(bb.y); }
    const int ks_p = tb + 32 * (w - 4); const bool do_p = w >= 4 && w < 7 && ks_p < 68;
    const bool p_w2 = ks_p < 48;
    unsigned rp[5];
#pragma unroll
    for (int u = 0; u < 5; ++u) { const int L = 64 * u + lane; rp[u] = (L / 10) * 1280 + 160 * g + (L % 10) * 16; }
    { unsigned keep, sf, spin; u64 tmp;
      asm volatile(A2_ASM
        : [acc0] "=&v"(acc0), [acc1] "=&v"(acc1), [keep] "=&s"(keep), [sf] "=&s"(sf), [spin] "=&s"(spin), [tmp] "=&s"(tmp),
          "=&{v[128:143]}"(kv0), "=&{v[144:147]}"(kv1), "+{s40}"(kvo)
        : [bias] "v"(bias), [va] "v"(lds0 + RING + (8 * h + q4) * 384 + (phi ^ sw) * 64 + blk * 32 + pp * 8),
          [va2] "v"(lds0 + RING + 2 * SLOT + (8 * h + q4) * 384 + (phi ^ sw) * 64 + blk * 32 + pp * 8), [vb] "v"(lds0 + (32 * tt + r) * XS_STR + h * 16),
          [of0] "v"(offF[0]), [of1] "v"(offF[1]), [of2] "v"(offF[2]), [w1r] "s"(w1r), [sd] "s"(lds0 + RING + w * 3072), [wid] "s"(w),
          [tagv] "v"(tagv), [fst] "v"(WS_FKV + (lane & 7) * 2048 + (tb * 8 + g) * 8), [fld] "v"(WS_FKV + (b & 7) * 2048 + ((w & 3) * 64 + lane) * 8),
          [a16] "v"(lane * 16), [smp] "s"(lds0 + MISC), [dum] "s"(lds0 + MISC + 6144), [shr] "s"(b >> 3),
          [dop] "s"(__builtin_amdgcn_readfirstlane((int)do_p)), [rp0] "v"(rp[0]), [rp1] "v"(rp[1]), [rp2] "v"(rp[2]), [rp3] "v"(rp[3]), [rp4] "v"(rp[4]),
          [rpr] "s"(p_w2 ? MKRS(w2, 1966080) : MKRS(wo, 819200)), [rps] "s"(__builtin_amdgcn_readfirstlane((p_w2 ? ks_p : ks_p - 48) * 40960)), [rpd] "s"(lds0 + RING + (w & 3) * 5120),
          "{s[56:59]}"(wsr), "{s[46:47]}"(tag)
        : "v150", "v176", "v177", "v178", "v179", "v180", "v181", "v182", "v183", "v184", "v185", "v186", "v187", "v188", "v189", "v190", "v191", "v192", "v193", "v194", "v195", "v196", "v197", "v198", "v199", "v200", "v201", "v202", "v203", "v204", "v205", "v206", "v207", "v208", "v209", "v210", "v211", "v212", "v213", "v214", "v215", "v216", "v217", "v218", "v219", "v220", "v221", "v222", "v223", "v148", "v149", "vcc", "scc", "memory" ASM_ST_CLOB); ASM_ST_OUT(2);
    }
    STAMP(4);
    auto put_tile = [&](const f32x16& a, int t2) {
      unsigned pkd[8];
#pragma unroll
      for (int i = 0; i < 8; ++i) { const float x0 = a[2 * i], x1 = a[2 * i + 1]; pkd[i] = cvtpk(x0 * fmaxf(x0, 0.f), x1 * fmaxf(x1, 0.f)); }
      const unsigned hf = WS_HF + ((tb * 4 + 2 * t2 + (r >> 4)) * 48 + 6 * g + phi) * 1024 + (h * 16 + (r & 15)) * 16;
      ST16((u32x4{pkd[0], pkd[1], pkd[4], pkd[5]}), wsr, hf);
      ST16((u32x4{pkd[2], pkd[3], pkd[6], pkd[7]}), wsr, hf + 512);
    };
    if (P) { put_tile(acc0, 0); put_tile(acc1, 1); } else put_tile(acc0, tt);
    asm volatile("s_cmp_ge_u32 %2, 4\n\ts_cbranch_scc1 1f\n\ts_waitcnt vmcnt(4)\n\ts_branch 2f\n1:\n\ts_waitcnt vmcnt(2)\n2:"
                 : "+{v[128:143]}"(kv0), "+{v[144:147]}"(kv1) : "s"(w) : "scc", "memory");
    if (do_p) {
      const lptr tp = lds + RING + (w & 3) * 5120 + (4 * (lane >> 4) + ((lane & 15) >> 2)) * 160 + (lane & 3) * 8;
#pragma unroll
      for (int nu = 0; nu < 5; ++nu) { const s16x4 lo = trd(tp + nu * 32), hi = trd(tp + nu * 32 + 16 * 160);
        ST16(__builtin_bit_cast(u32x4, cat8(lo, hi)), wsr, WS_WP + ((g * 68 + ks_p) * 5 + nu) * 1024 + lane * 16); }
    }
    if (tid < 8) *(LAS int*)(lds + 161536 + tid * 4) = 0;
    BAR();
  }
  STAMP(5);
  STAMP_OUT2(0, 6);
  if (STOP_AFTER <= 2) return;

  if (b == 0 && tid == 0) ST8(tagv, wsr, WS_E);
  u32x16 hq0, hq1, hq2, hq3, hq4; u32x8 hq5;
  {
    const int kq = w & 3, rh = w >> 2;
    int ln = lane;
    int attempt = 0;
#pragma unroll 1
    for (;; ++attempt) {
      asm volatile("" : "+s"(attempt));
      const int hh_ = ln >> 5;
      const unsigned a16 = ln * 16;
      if (attempt) {
        float m[4] = {-INFINITY, -INFINITY, -INFINITY, -INFINITY};
#pragma unroll 1
        for (int t = 0; t < 64; ++t) {
          const bf16x8 ka = __builtin_bit_cast(bf16x8, LD16(wsr, WS_KV + t * KV_TILE + a16)); const s16x4 kb = __builtin_bit_cast(s16x4, LD8(wsr, WS_KV + t * KV_TILE + 3072 + a16));
#pragma unroll
          for (int i = 0; i < 4; ++i) {
            f32x16 sv = {0, 0, 0, 0, 0, 0, 0, 0, 0, 0, 0, 0, 0, 0, 0, 0};
            const u32x4 qa4 = {QAv[4 * i], QAv[4 * i + 1], QAv[4 * i + 2], QAv[4 * i + 3]};
            const u32x2 qb2 = {hh_ ? 0u : QBv[2 * i], hh_ ? 0u : QBv[2 * i + 1]};
            sv = MF32(ka, __builtin_bit_cast(bf16x8, qa4), sv);
            sv = __builtin_amdgcn_mfma_f32_32x32x8bf16_1k(kb, __builtin_bit_cast(s16x4, qb2), sv, 0, 0, 0);
            m[i] = fmaxf(m[i], max16(sv));
          }
        }
#pragma unroll
        for (int i = 0; i < 4; ++i) {
          const auto sw = __builtin_amdgcn_permlane32_swap(__float_as_uint(m[i]), __float_as_uint(m[i]), false, false);
          const float nm = -fmaxf(__uint_as_float(sw[0]), __uint_as_float(sw[1]));
          const unsigned u1 = __float_as_uint(nm) & 0xffff0000u; const float r1 = nm - __uint_as_float(u1);
          const unsigned u2 = __float_as_uint(r1) & 0xffff0000u; const float r2 = r1 - __uint_as_float(u2);
          const unsigned u3 = __float_as_uint(r2) & 0xffff0000u;
          if (hh_) { QBv[2 * i] = (u1 >> 16) | u2; QBv[2 * i + 1] = u3 >> 16; }
        }
      }
      f32x16 o0, o1, o2, o3;
      const unsigned flo = WS_FC + (tb * 8 + g) * 1024;
      unsigned ho0 = WS_HF + ((tb * 4 + 2 * (w & 1)) * 48 + 12 * (w >> 1)) * 1024, ho1 = ho0 + 49152;
      const int it0 = w * 64 + ln, it1 = min(it0 + 512, 639);
      unsigned wpo = WS_WP + (g * 68 + 12 * (w & 3) + (w >> 2)) * 5120;
      const unsigned wdst = lds0 + (w >> 2) * 20480 + (w & 3) * 5120;
      constexpr int NW = (2720 / SHARE + 7) / 8;
      const int wl = (sh * 8 + w) * NW + ln;
      const unsigned wwo = (ln < NW && wl < 2720) ? WS_WP + g * 68 * 5120 + wl * 128 : 0x7fffffffu;
      STAMP(6);
      asm volatile(ATTN_ASM
        : "={v[0:15]}"(o0), "={v[16:31]}"(o1), "={v[32:47]}"(o2), "={v[48:63]}"(o3),
          "={v[168:183]}"(hq0), "={v[184:199]}"(hq1), "={v[200:215]}"(hq2), "={v[216:231]}"(hq3), "={v[232:247]}"(hq4), "={v[248:255]}"(hq5),
          "+{s40}"(kvo), "+{s48}"(ho0), "+{s49}"(ho1), "+{s52}"(wpo), "+{v[120:127]}"(QBv), "+{v[128:143]}"(kv0), "+{v[144:147]}"(kv1)
        : "{v[104:119]}"(QAv), "{v156}"(a16), "{v157}"(wwo), "{s44}"(flo), "{s[46:47]}"(tag), "{s62}"(wdst), "{s63}"(lds0 + 161024), "{s[56:59]}"(wsr),
          "{s64}"(WS_FC + tb * 8192 + g * 16), "{s65}"(WS_FC + g * 1024 + (8 + tb) * 16 - 65536), "{s66}"(w),
          "{v160}"(lds0 + 161536 + w * 4), "{v162}"(lds0 + 161536 + (w ^ 4) * 4),
          "{s73}"(attempt ? 0u : lds0 + MISC), "{s74}"(-REF_OFF),
          "{s76}"(ho0 + 11264), "{s77}"(ho0 + 11264 + 49152), "{s78}"(tb * 64 * 1280 + 160 * g), "{s[80:83]}"(MKRS(x, 2621440)),
          "{v163}"((it0 / 10) * 1280 + (it0 % 10) * 16), "{v164}"((it1 / 10) * 1280 + (it1 % 10) * 16)
        : "v64","v65","v66","v67","v68","v69","v70","v71","v72","v73","v74","v75","v76","v77","v78","v79",
          "v80","v81","v82","v83","v84","v85","v86","v87","v88","v89","v90","v91","v92","v93","v94","v95",
          "v96","v97","v98","v99","v100","v101","v102","v103",
          "v148","v149","v150","v151","v152","v153","v154","v155",
          "v158","v159","v161","s42","s67","s68","s69","s70","s71","s72","s75","vcc","scc","memory" ASM_ST_CLOB); ASM_ST_OUT(4);
      STAMP(7);
      asm volatile("v_mbcnt_lo_u32_b32 %0, -1, 0\n\tv_mbcnt_hi_u32_b32 %0, -1, %0" : "=v"(ln));
      LAS int* redo = (LAS int*)(lds + 160000);
      if (attempt == 0 && w == 0 && ln == 0) *redo = 0;
      { const lptr base = lds + 81920 + rh * 12 * 3072 + ln * 16;
#define PUT(o, i) do { const lptr p_ = base + (((kq + (i)) & 3) * 3 + (i) - 1) * 3072; *(LAS f32x4*)(p_) = f32x4{o[0], o[1], o[2], o[3]}; \
          *(LAS f32x4*)(p_ + 1024) = f32x4{o[4], o[5], o[6], o[7]}; *(LAS f32x4*)(p_ + 2048) = f32x4{o[8], o[9], o[10], o[11]}; } while (0)
        PUT(o1, 1); PUT(o2, 2); PUT(o3, 3);
#undef PUT
      }
      unsigned oo[3];
      { const int h2 = ln >> 5, r2 = ln & 31;
        const unsigned lb = WS_OF + (tb * 4 + 2 * rh + (r2 >> 4)) * 20480 + (r2 & 15) * 16;
#pragma unroll
        for (int a = 0; a < 3; ++a) { const int c0 = 80 * g + 20 * kq + 8 * a, c1 = c0 + 4;
          const unsigned s0 = (c0 >> 5) * 1024 + ((c0 & 15) >> 2) * 256 + ((c0 >> 4) & 1) * 8, s1 = (c1 >> 5) * 1024 + ((c1 & 15) >> 2) * 256 + ((c1 >> 4) & 1) * 8;
          oo[a] = lb + (h2 ? s1 : s0); }
        if (h2) oo[2] = 0x7fffffffu;
      }
      BAR();
      f32x4 t[9];
      { const lptr base = lds + 81920 + (rh * 12 + kq * 3) * 3072 + ln * 16;
#pragma unroll
        for (int k2 = 0; k2 < 9; ++k2) t[k2] = *(LAS f32x4*)(base + k2 * 1024);
        SB(); }
      f32x4 oc[3];
#pragma unroll
      for (int c = 0; c < 3; ++c) oc[c] = (t[c] + t[3 + c]) + (t[6 + c] + f32x4{o0[4 * c], o0[4 * c + 1], o0[4 * c + 2], o0[4 * c + 3]});
      const auto sw = __builtin_amdgcn_permlane32_swap(__float_as_uint(oc[2][0]), __float_as_uint(oc[2][0]), false, false);
      const float l = __uint_as_float(sw[1]), rl = __builtin_amdgcn_rcpf(l);
      const f32x4 zz = (oc[0] * 0.f + oc[1] * 0.f) + oc[2] * 0.f; const float z = (zz[0] + zz[1]) + (zz[2] + zz[3]);
      const bool bad = !(l > 0x1p-110f && l < 0x1p120f && z == 0.f);
      if (attempt == 0 && __any(bad)) *redo = 1;
#pragma unroll
      for (int a = 0; a < 3; ++a) { const f32x4 v = oc[a] * rl; ST8((u32x2{cvtpk(v[0], v[1]), cvtpk(v[2], v[3])}), wsr, oo[a]); }
      BAR();
      if (attempt != 0 || *redo == 0) break;
      BAR();
      kvo = WS_KV + kq * 16 * KV_TILE;
      asm volatile(ATTN_PRE "s_waitcnt vmcnt(0)" : "=&{v[128:143]}"(kv0), "=&{v[144:147]}"(kv1), "+{s40}"(kvo) : "{v156}"(ln * 16), "{s[56:59]}"(wsr) : "scc", "memory");
    }
    STAMP(8);
  }
  if (STOP_AFTER <= 3) { DRAIN(); STAMP_OUT(9); return; }

  {
    int ln; asm volatile("v_mbcnt_lo_u32_b32 %0, -1, 0\n\tv_mbcnt_hi_u32_b32 %0, -1, %0" : "=v"(ln));
    const int th = w & 1, kq = w >> 1;
    const u32x2 tg = {(unsigned)tag, (unsigned)(tag >> 32)};
    const unsigned a16 = ln * 16;
    const int it0 = w * 64 + ln, it1 = min(it0 + 512, 639);
    f32x16 ac0, ac1; f32x8 ac2; u32x8 bb;
    { unsigned keep, so4, spin; u64 tmp;
      unsigned so = WS_WP + (g * 68 + 12 * (w & 3) + 4 + (w >> 2)) * 5120;
      const unsigned apl = lds0 + kq * 5120 + a16;
      STAMP(9);
      asm volatile(C_ASM
        : "=&{v[0:15]}"(ac0), "=&{v[16:31]}"(ac1), "=&{v[32:39]}"(ac2), "=&{v[120:127]}"(bb),
          [keep] "=&s"(keep), [so4] "=&s"(so4), [spin] "=&s"(spin), [tmp] "=&s"(tmp), [so] "+s"(so)
        : "{v[168:183]}"(hq0), "{v[184:199]}"(hq1), "{v[200:215]}"(hq2), "{v[216:231]}"(hq3), "{v[232:247]}"(hq4), "{v[248:255]}"(hq5),
          "{v[128:143]}"(kv0), "{s[56:59]}"(wsr), "{s[46:47]}"(tag),
          [a16] "v"(a16), [apl] "v"(apl), [apm] "v"(apl + 61440), [aph] "v"(apl + 122880), [tagv] "v"(tg),
          [fst] "v"(WS_FO + (tb * 8 + (ln & 7)) * 1024 + (g * 8 + w) * 16),
          [bo0] "v"(160 * g + (it0 % 10) * 16), [bo1] "v"(160 * g + (it1 % 10) * 16),
          [so2] "s"(WS_WP + (g * 68 + 48 + 5 * (w & 3) + (w >> 2)) * 5120), [sd] "s"(lds0 + (w >> 2) * 20480 + (w & 3) * 5120),
          [flo] "s"(WS_FO + (tb * 8 + g) * 1024), [ofo] "s"(WS_OF + ((tb * 4 + 2 * th) * 20 + 5 * kq) * 1024),
          [wid] "s"(w), [lo4] "s"((w - 4) >> 31), [b2r] "s"(MKRS(b2, 1280))
        : "v40", "v41", "v42", "v43", "v44", "v45", "v46", "v47", "v48", "v49", "v50", "v51", "v52", "v53", "v54", "v55", "v56", "v57", "v58", "v59", "v60", "v61", "v62", "v63", "v64", "v65", "v66", "v67", "v68", "v69", "v70", "v71", "v72", "v73", "v74", "v75", "v76", "v77", "v78", "v79", "v80", "v81", "v82", "v83", "v84", "v85", "v86", "v87", "v88", "v89", "v90", "v91", "v92", "v93", "v94", "v95", "v96", "v97", "v98", "v99", "v100", "v101", "v102", "v103", "v104", "v105", "v106", "v107", "v108", "v109", "v110", "v111", "v112", "v113", "v114", "v115", "v116", "v117", "v118", "v119", "v148", "v149", "vcc", "scc", "memory" ASM_ST_CLOB); ASM_ST_OUT(3);
    }
    f32x4 acc[5][2];
#pragma unroll
    for (int e = 0; e < 4; ++e) { acc[0][0][e] = ac0[e]; acc[0][1][e] = ac0[4 + e]; acc[1][0][e] = ac0[8 + e]; acc[1][1][e] = ac0[12 + e];
      acc[2][0][e] = ac1[e]; acc[2][1][e] = ac1[4 + e]; acc[3][0][e] = ac1[8 + e]; acc[3][1][e] = ac1[12 + e]; acc[4][0][e] = ac2[e]; acc[4][1][e] = ac2[4 + e]; }
    const u32x4 xr0 = {kv0[12], kv0[13], kv0[14], kv0[15]}, xr1 = kv1, bb0 = {bb[0], bb[1], bb[2], bb[3]}, bb1 = {bb[4], bb[5], bb[6], bb[7]};
    STAMP(10);
    const lptr pw = lds + kq * 21504 + (32 * th + (ln & 15)) * 336 + (ln >> 4) * 16;
    const int t0 = it0 / 10, p0 = it0 - 10 * t0, t1 = it1 / 10, p1 = it1 - 10 * t1;
    const rsrc_t outr = MKRS(out, 2621440);
    const unsigned ob = tb * 64 * 1280 + 160 * g;
    BAR();
#pragma unroll
    for (int n = 0; n < 5; ++n)
#pragma unroll
      for (int j = 0; j < 2; ++j) *(LAS f32x4*)(pw + j * 16 * 336 + n * 64) = acc[n][j];
    BAR();
    auto finish = [&](int t, int p, const u32x4 xr, const u32x4 bb) {
      const lptr pr = lds + t * 336 + p * 32;
      f32x4 q[8];
#pragma unroll
      for (int k2 = 0; k2 < 4; ++k2) { q[2 * k2] = *(LAS f32x4*)(pr + k2 * 21504); q[2 * k2 + 1] = *(LAS f32x4*)(pr + k2 * 21504 + 16); }
      const f32x4 r0 = f32x4{bf_lo(xr.x), bf_hi(xr.x), bf_lo(xr.y), bf_hi(xr.y)} + f32x4{bf_lo(bb.x), bf_hi(bb.x), bf_lo(bb.y), bf_hi(bb.y)};
      const f32x4 r1 = f32x4{bf_lo(xr.z), bf_hi(xr.z), bf_lo(xr.w), bf_hi(xr.w)} + f32x4{bf_lo(bb.z), bf_hi(bb.z), bf_lo(bb.w), bf_hi(bb.w)};
      SB();
      const f32x4 s0 = ((q[0] + q[2]) + (q[4] + q[6])) + r0, s1 = ((q[1] + q[3]) + (q[5] + q[7])) + r1;
      __builtin_amdgcn_raw_buffer_store_b128((u32x4{cvtpk(s0[0], s0[1]), cvtpk(s0[2], s0[3]), cvtpk(s1[0], s1[1]), cvtpk(s1[2], s1[3])}), outr, t * 1280 + p * 16, ob, 0);
    };
    finish(t0, p0, xr0, bb0);
    if (w < 2) finish(t1, p1, xr1, bb1);
  }
  STAMP(11);
  STAMP_OUT(12);
}

extern "C" void kernel_launch(void* const* d_in, const int* in_sizes, int n_in, void* d_out, int out_size, void* d_ws, size_t ws_size, hipStream_t stream) {
  (void)in_sizes; (void)n_in; (void)out_size; (void)ws_size;
  (void)hipFuncSetAttribute((const void*)fused_block, hipFuncAttributeMaxDynamicSharedMemorySize, LDS_BYTES);
  fused_block<<<256, 512, LDS_BYTES, stream>>>((const u16*)d_in[0], (const u16*)d_in[1], (const u16*)d_in[2], (const u16*)d_in[3], (const u16*)d_in[4],
                                               (const u16*)d_in[6], (const u16*)d_in[7], (const u16*)d_in[8], (const u16*)d_in[9], (u16*)d_out, (char*)d_ws);
}
```
